```python
import torch

def _softmax(x, axis=-1):
    return torch.softmax(x, dim=axis)
def _gelu(x):
    return torch.nn.functional.gelu(x, approximate="tanh")
def _silu(x):
    return torch.nn.functional.silu(x)
def _sigmoid(x):
    return torch.sigmoid(x)
def _layernorm(x, axis=-1, eps=1e-5):
    m = x.mean(dim=axis, keepdim=True)
    v = x.var(dim=axis, keepdim=True, unbiased=False)
    return (x - m) / torch.sqrt(v + eps)
def _scatter_add(tgt, idx, src, axis=0):
    return tgt.clone().index_add_(axis, idx, src)
def _segment_sum(src, seg, n):
    out = torch.zeros((n,) + src.shape[1:], dtype=src.dtype)
    return out.index_add_(0, seg, src)
def _pad(x, pw):
    flat = tuple(v for pair in reversed(pw) for v in pair)
    return torch.nn.functional.pad(x, flat)

kernel_name = 'interleave_layers__softmax_full_mha__sliding_local_dilated__prompt'

def setup_inputs():

    _w = (lambda a: (lambda t: t.to(torch.bfloat16) if t.is_floating_point() else t)(torch.as_tensor(a)))
    def _A_setup():
        B, S, D = 1, 2048, 640
        return {
            "x": _w((lambda *s: torch.randn(*s, dtype=torch.bfloat16) * 0.05)(B, S, D)),
            "wqkv": _w((lambda *s: torch.randn(*s, dtype=torch.bfloat16) * 0.05)(D, 3 * D)),
            "wo": _w((lambda *s: torch.randn(*s, dtype=torch.bfloat16) * 0.05)(D, D)),
        }
    _A_in = _A_setup()

    def _B_setup():
        B, S, D = 1, 2048, 640
        return {
            "x": _w((lambda *s: torch.randn(*s, dtype=torch.bfloat16) * 0.05)(B, S, D)),
            "wqkv": _w((lambda *s: torch.randn(*s, dtype=torch.bfloat16) * 0.05)(D, 3 * D)),
            "wo": _w((lambda *s: torch.randn(*s, dtype=torch.bfloat16) * 0.05)(D, D)),
        }
    _B_in = _B_setup()

    return {
        "A_x": _A_in["x"],
        "A_wqkv": _A_in["wqkv"],
        "A_wo": _A_in["wo"],
        "B_x": _B_in["x"],
        "B_wqkv": _B_in["wqkv"],
        "B_wo": _B_in["wo"],
    }

def reference(A_x, A_wqkv, A_wo, B_x, B_wqkv, B_wo):
    def _A_ref(x, wqkv, wo):
        B, S, D = x.shape
        H = 32; Dh = D // H
        qkv = (lambda x, *d: x.permute(*d))((x @ wqkv).reshape(B, S, 3, H, Dh), 2, 0, 3, 1, 4)
        q, k, v = qkv[0], qkv[1], qkv[2]
        scale = Dh ** -0.5
        scores = torch.einsum("bhqd,bhkd->bhqk", q, k) * scale
        p = _softmax(scores, axis=-1)
        o = torch.einsum("bhqk,bhkd->bhqd", p, v)
        return (lambda x, *d: x.permute(*d))(o, 0, 2, 1, 3).reshape(B, S, D) @ wo

    def _B_ref(x, wqkv, wo):
        B, S, D = x.shape
        H = 32; Dh = D // H
        W, dil = 8, 2
        qkv = (lambda x, *d: x.permute(*d))((x @ wqkv).reshape(B, S, 3, H, Dh), 2, 0, 3, 1, 4)
        q, k, v = qkv[0], qkv[1], qkv[2]
        scale = Dh ** -0.5
        pos = torch.arange(S)
        off = torch.arange(W) * dil
        idx = pos[:, None] - off[None, :]
        mask = idx >= 0
        idx_c = torch.where(mask, idx, 0).reshape(-1)
        kg = k[:, :, idx_c, :].reshape(B, H, S, W, Dh)
        vg = v[:, :, idx_c, :].reshape(B, H, S, W, Dh)
        logits = torch.einsum("bhsd,bhswd->bhsw", q, kg) * scale
        logits = torch.where(mask[None, None, :, :], logits, -1e9)
        attn = _softmax(logits, axis=-1)
        o = torch.einsum("bhsw,bhswd->bhsd", attn, vg)
        return (lambda x, *d: x.permute(*d))(o, 0, 2, 1, 3).reshape(B, S, D) @ wo

    x = A_x
    x = x + _A_ref(_layernorm(x, axis=-1), A_wqkv, A_wo)
    x = x + _B_ref(_layernorm(x, axis=-1), B_wqkv, B_wo)
    x = x + _A_ref(_layernorm(x, axis=-1), A_wqkv, A_wo)
    x = x + _B_ref(_layernorm(x, axis=-1), B_wqkv, B_wo)
    return x
```

```cpp
#include <hip/hip_runtime.h>
#include <stdint.h>

#define DI __device__ __forceinline__
typedef short bf16x8 __attribute__((ext_vector_type(8)));
typedef short s16x4 __attribute__((ext_vector_type(4)));
typedef short v4i16_t __attribute__((ext_vector_type(4)));
typedef float f32x16 __attribute__((ext_vector_type(16)));
typedef float f32x4 __attribute__((ext_vector_type(4)));
typedef float f32x2 __attribute__((ext_vector_type(2)));
typedef unsigned u32x2 __attribute__((ext_vector_type(2)));
typedef unsigned u32x4 __attribute__((ext_vector_type(4)));
typedef __bf16 bf16x2_t __attribute__((ext_vector_type(2)));
#define LAS __attribute__((address_space(3)))
typedef LAS char lchar;
typedef __amdgpu_buffer_rsrc_t rsrc_t;
#define SBAR() __builtin_amdgcn_sched_barrier(0)
#define PIN(x) asm volatile("" : "+v"(x))
#define MFMA32(a, b, c) __builtin_amdgcn_mfma_f32_32x32x16_bf16(a, b, c, 0, 0, 0)
#define MFMA8(a, b, c) __builtin_amdgcn_mfma_f32_32x32x8bf16_1k(a, b, c, 0, 0, 0)
#define MFMA16(a, b, c) __builtin_amdgcn_mfma_f32_16x16x32_bf16(a, b, c, 0, 0, 0)
#define SC1 16

DI unsigned pkbf(float a, float b) { bf16x2_t v = {(__bf16)a, (__bf16)b}; return __builtin_bit_cast(unsigned, v); }
DI float bflo(unsigned u) { return __uint_as_float(u << 16); }
DI float bfhi(unsigned u) { return __uint_as_float(u & 0xffff0000u); }
DI float ex2(float x) { return __builtin_amdgcn_exp2f(x); }
DI float dot2(unsigned a, unsigned b, float c) { return __builtin_amdgcn_fdot2_f32_bf16(__builtin_bit_cast(bf16x2_t, a), __builtin_bit_cast(bf16x2_t, b), c, false); }
DI bf16x8 lds_b128(const lchar* p) { return *(const LAS bf16x8*)p; }
DI s16x4 lds_b64(const lchar* p) { return *(const LAS s16x4*)p; }
DI s16x4 lds_tr(const lchar* p) { return __builtin_bit_cast(s16x4, __builtin_amdgcn_ds_read_tr16_b64_v4i16((LAS v4i16_t*)p)); }
DI bf16x8 cat(s16x4 a, s16x4 b) { return __builtin_shufflevector(a, b, 0, 1, 2, 3, 4, 5, 6, 7); }
DI float swap32_sum(float v) { auto r = __builtin_amdgcn_permlane32_swap(__float_as_uint(v), __float_as_uint(v), false, false); return __uint_as_float(r[0]) + __uint_as_float(r[1]); }
DI float swap32_max(float v) { auto r = __builtin_amdgcn_permlane32_swap(__float_as_uint(v), __float_as_uint(v), false, false); return __builtin_fmaxf(__uint_as_float(r[0]), __uint_as_float(r[1])); }
DI float swap16_sum(float v) { auto r = __builtin_amdgcn_permlane16_swap(__float_as_uint(v), __float_as_uint(v), false, false); return __uint_as_float(r[0]) + __uint_as_float(r[1]); }

constexpr int S = 2048, D = 640;
constexpr float QSCALE = 0.22360679774997896f * 1.4426950408889634f;
constexpr unsigned XB = S * D * 2;
constexpr int KVSTAGE = 12288;
constexpr int KVK = 0, KVV = 6144, ROUND = 2 * KVSTAGE;
constexpr unsigned QBYTES = 32 * 64 * 1536, KVBYTES = 32 * 16 * KVSTAGE;
constexpr unsigned WS_X = 0;
constexpr unsigned WS_O = WS_X + 3 * XB;
constexpr unsigned WS_Q = WS_O + 4 * XB;
constexpr unsigned WS_KV = WS_Q + 2 * QBYTES;
constexpr unsigned WS_END = WS_KV + 2 * KVBYTES;

struct Params { const char* x0; const char* wqA; const char* woA; const char* wqB; const char* woB; char* out; char* ws; };

DI void dma1(unsigned voff, const char* sbase, unsigned ldsdst) {
  unsigned keep;
  asm volatile("s_mov_b32 %0, m0\n\ts_mov_b32 m0, %3\n\ts_nop 2\n\tglobal_load_lds_dwordx4 %1, %2 sc1\n\ts_mov_b32 m0, %0"
               : "=&s"(keep) : "v"(voff), "s"(sbase), "s"(ldsdst) : "memory");
}
DI void dma1m(unsigned voff, const char* sbase, unsigned ldsdst, unsigned long long mask) {
  unsigned keep;
  asm volatile("s_mov_b32 %0, m0\n\ts_mov_b32 m0, %3\n\ts_mov_b64 exec, %4\n\ts_nop 1\n\tglobal_load_lds_dwordx4 %1, %2 sc1\n\ts_mov_b64 exec, -1\n\ts_mov_b32 m0, %0"
               : "=&s"(keep) : "v"(voff), "s"(sbase), "s"(ldsdst), "s"(mask) : "memory");
}
DI void dma3(unsigned voff, const char* sbase, unsigned ldsdst) {
  unsigned keep;
  asm volatile("s_mov_b32 %0, m0\n\ts_mov_b32 m0, %3\n\ts_nop 2\n\t"
               "global_load_lds_dwordx4 %1, %2 sc1\n\tglobal_load_lds_dwordx4 %1, %2 offset:1024 sc1\n\tglobal_load_lds_dwordx4 %1, %2 offset:2048 sc1\n\t"
               "s_mov_b32 m0, %0" : "=&s"(keep) : "v"(voff), "s"(sbase), "s"(ldsdst) : "memory");
}
DI void dma4(unsigned voff, const char* sbase, unsigned ldsdst) {
  unsigned keep;
  asm volatile("s_mov_b32 %0, m0\n\ts_mov_b32 m0, %3\n\ts_nop 2\n\t"
               "global_load_lds_dwordx4 %1, %2 sc1\n\tglobal_load_lds_dwordx4 %1, %2 offset:1024 sc1\n\tglobal_load_lds_dwordx4 %1, %2 offset:2048 sc1\n\tglobal_load_lds_dwordx4 %1, %2 offset:3072 sc1\n\t"
               "s_mov_b32 m0, %0" : "=&s"(keep) : "v"(voff), "s"(sbase), "s"(ldsdst) : "memory");
}
template <int N> DI void wait_vm() { asm volatile("s_waitcnt vmcnt(%0)" :: "i"(N) : "memory"); }
DI void bar() { asm volatile("s_barrier" ::: "memory"); }
DI void bar_lgkm() { asm volatile("s_waitcnt lgkmcnt(0)\n\ts_barrier" ::: "memory"); }

constexpr int QSLOT = 34816, QA = 0, QW = 16384, QH = 32768;
constexpr int T_SQ = 4 * QSLOT, T_SX = T_SQ + 640, T_CS = T_SX + 640;
constexpr int L_QK = 0, L_V = 20 * 144 * 8;

template <bool BL, bool FIRST>
DI void phase_qkv(lchar* lds, const Params& p, const char* W, unsigned xoff, unsigned qoff, unsigned kvoff, unsigned ooff, int bx, int bj) {
  const int tid = threadIdx.x, lane = tid & 63, w = __builtin_amdgcn_readfirstlane(tid >> 6);
  const unsigned lds0 = (unsigned)(uintptr_t)lds;
  const int rt = 2 * bx + (bj & 1), hp = bj >> 1;
  const int h = lane >> 5, l31 = lane & 31;
  rsrc_t rs = __builtin_amdgcn_make_buffer_rsrc((void*)p.ws, 0, WS_END, 0x00020000);
  constexpr int NA = BL ? 5 : 4, NW = 4;
  if (tid < 256) { const int sl = tid >> 6, row = tid & 63; const int pc = 15 ^ (((row & 3) << 2) | ((row >> 2) & 3));
    *(LAS u32x4*)(lds + sl * QSLOT + QW + row * 256 + pc * 16) = (u32x4){0x3f803f80u, 0x3f803f80u, 0x3f803f80u, 0x3f803f80u}; }
  f32x16 acc[2][2];
  f32x4 hacc[2];
  float ssq = 0.f, csum = 0.f;
  if (w >= 4) {
    const int lw = w - 4;
    unsigned wv[4]; unsigned long long wm[4];
#pragma unroll
    for (int i = 0; i < 4; ++i) { const int q = lane >> 4, c = (lane & 15) ^ ((q << 2) | i); const int cc = c == 15 ? 14 : c;
      wv[i] = q * 3840 + (cc / 5) * 1280 + 80 * hp + (cc % 5) * 16; wm[i] = __ballot(c != 15); }
    auto issueW = [&](int t) {
#pragma unroll
      for (int i = 0; i < 4; ++i) dma1m(wv[i], W + (size_t)(64 * t + 16 * lw + 4 * i) * 3840, lds0 + (t & 3) * QSLOT + QW + (16 * lw + 4 * i) * 256, wm[i]); };
    unsigned av0, av1;
    if (FIRST) { av0 = (lane >> 3) * 1280 + (((lane & 7) ^ (lane >> 4)) * 16); av1 = (lane >> 3) * 1280 + (((lane & 7) ^ (4 + (lane >> 4))) * 16); }
    else { av0 = lane * 16; av1 = 0; }
    const int htb = rt == 0 ? 0 : 4 * rt - 1;
    const unsigned hv = (lane >> 3) * 256 + 128 + (lane & 7) * 16;
    auto issueA = [&](int t) {
      if (FIRST) {
#pragma unroll
        for (int i = 0; i < 4; ++i) dma1((i & 1) ? av1 : av0, p.x0 + (size_t)(128 * rt + 32 * lw + 8 * i) * 1280 + t * 128, lds0 + (t & 3) * QSLOT + QA + lw * 4096 + i * 1024);
      } else dma4(av0, p.ws + xoff + (size_t)(4 * rt + lw) * 40960 + t * 4096, lds0 + (t & 3) * QSLOT + QA + lw * 4096);
      if (BL) dma1m(hv, p.ws + xoff + (size_t)htb * 40960 + (16 * t + 4 * lw) * 256, lds0 + (t & 3) * QSLOT + QH + lw * 512, 0xffffffffull);
    };
    issueW(0); issueW(1); issueW(2); issueW(3);
    issueA(0); issueA(1); issueA(2); issueA(3);
    const int g = lane >> 4, q4 = (lane & 15) >> 2, p4 = lane & 3;
    const lchar* hx = lds + QH + 2 * g * 128 + (lane & 15) * 8;
    const lchar* hw[2][2];
#pragma unroll
    for (int n = 0; n < 2; ++n)
#pragma unroll
      for (int tt = 0; tt < 2; ++tt) { const int row = 8 * g + 4 * tt + q4, ch = 2 * (2 * lw + n) + (p4 >> 1);
        hw[n][tt] = lds + QW + row * 256 + (ch ^ (((row & 3) << 2) | ((row >> 2) & 3))) * 16 + 8 * (p4 & 1); }
    hacc[0] = (f32x4){0.f, 0.f, 0.f, 0.f}; hacc[1] = hacc[0];
#define LSTAGE(T, N) do { wait_vm<N>(); bar_lgkm(); if ((T) >= 1 && (T) + 3 <= 9) { issueA((T) + 3); issueW((T) + 3); } \
      if (BL) { _Pragma("unroll") for (int s = 0; s < 2; ++s) { const int so = ((T) & 3) * QSLOT; \
        const bf16x8 xf = cat(lds_b64(hx + so + s * 1024), lds_b64(hx + so + s * 1024 + 128)); const u32x4 xu = __builtin_bit_cast(u32x4, xf); \
        ssq = dot2(xu[0], xu[0], ssq); ssq = dot2(xu[1], xu[1], ssq); ssq = dot2(xu[2], xu[2], ssq); ssq = dot2(xu[3], xu[3], ssq); \
        _Pragma("unroll") for (int n = 0; n < 2; ++n) hacc[n] = MFMA16(cat(lds_tr(hw[n][0] + so + s * 8192), lds_tr(hw[n][1] + so + s * 8192)), xf, hacc[n]); } } } while (0)
    LSTAGE(0, 3 * NA); LSTAGE(1, 2 * NA); LSTAGE(2, 2 * NA + NW); LSTAGE(3, 2 * (NA + NW)); LSTAGE(4, 2 * (NA + NW));
    LSTAGE(5, 2 * (NA + NW)); LSTAGE(6, 2 * (NA + NW)); LSTAGE(7, 2 * (NA + NW)); LSTAGE(8, NA + NW); LSTAGE(9, 0);
    if (BL) {
      ssq = swap32_sum(swap16_sum(ssq));
      if (lw == 0 && lane < 16) *(LAS float*)(lds + T_SQ + lane * 4) = ssq;
      if (lw == 3 && lane >= 32 && lane < 48) *(LAS float*)(lds + T_SX + (lane - 32) * 4) = hacc[1][0];
    }
  } else {
    const int wm = w >> 1, wn = w & 1;
    const lchar* xa[4];
    if (FIRST) {
#pragma unroll
      for (int ks = 0; ks < 4; ++ks) xa[ks] = lds + QA + (64 * wm + l31) * 128 + (((2 * ks + h) ^ ((l31 >> 1) & 7)) * 16);
    } else { xa[0] = lds + QA + wm * 8192 + h * 512 + l31 * 8; xa[1] = xa[2] = xa[3] = xa[0]; }
    const int blk = (lane >> 4) & 1, q4 = (lane & 15) >> 2, p4 = lane & 3;
    const lchar* wa[2][2];
#pragma unroll
    for (int nt = 0; nt < 2; ++nt)
#pragma unroll
      for (int tt = 0; tt < 2; ++tt) { const int row = 8 * h + 4 * tt + q4, ch = 4 * (2 * wn + nt) + 2 * blk + (p4 >> 1);
        wa[nt][tt] = lds + QW + row * 256 + (ch ^ (((row & 3) << 2) | ((row >> 2) & 3))) * 16 + 8 * (p4 & 1); }
#pragma unroll
    for (int a = 0; a < 2; ++a)
#pragma unroll
      for (int b = 0; b < 2; ++b) acc[a][b] = (f32x16){};
    bf16x8 xf[2][2], wf[2][2];
#define CLOAD(BUF, T, KS) do { const int so_ = ((T) & 3) * QSLOT; \
      _Pragma("unroll") for (int mt = 0; mt < 2; ++mt) { \
        if (FIRST) xf[BUF][mt] = lds_b128(xa[KS] + so_ + mt * 4096); \
        else xf[BUF][mt] = cat(lds_b64(xa[0] + so_ + mt * 4096 + (KS) * 1024), lds_b64(xa[0] + so_ + mt * 4096 + (KS) * 1024 + 256)); } \
      _Pragma("unroll") for (int nt = 0; nt < 2; ++nt) wf[BUF][nt] = cat(lds_tr(wa[nt][0] + so_ + (KS) * 4096), lds_tr(wa[nt][1] + so_ + (KS) * 4096)); } while (0)
#define CMMA(BUF) do { \
      acc[0][0] = MFMA32(wf[BUF][0], xf[BUF][0], acc[0][0]); acc[0][1] = MFMA32(wf[BUF][1], xf[BUF][0], acc[0][1]); \
      acc[1][0] = MFMA32(wf[BUF][0], xf[BUF][1], acc[1][0]); acc[1][1] = MFMA32(wf[BUF][1], xf[BUF][1], acc[1][1]); \
      const u32x4 xu_ = __builtin_bit_cast(u32x4, wn ? xf[BUF][1] : xf[BUF][0]), wu_ = __builtin_bit_cast(u32x4, wm ? wf[BUF][1] : wf[BUF][0]); \
      _Pragma("unroll") for (int i = 0; i < 4; ++i) { ssq = dot2(xu_[i], xu_[i], ssq); csum = dot2(wu_[i], 0x3f803f80u, csum); } } while (0)
    bar_lgkm();
    CLOAD(0, 0, 0); SBAR();
#define CSTAGE(T) do { \
      CLOAD(1, T, 1); SBAR(); CMMA(0); SBAR(); \
      CLOAD(0, T, 2); SBAR(); CMMA(1); SBAR(); \
      CLOAD(1, T, 3); SBAR(); CMMA(0); SBAR(); \
      if ((T) < 9) { bar_lgkm(); CLOAD(0, (T) + 1, 0); SBAR(); } CMMA(1); SBAR(); } while (0)
    CSTAGE(0); CSTAGE(1); CSTAGE(2); CSTAGE(3); CSTAGE(4); CSTAGE(5); CSTAGE(6); CSTAGE(7); CSTAGE(8); CSTAGE(9);
    ssq = swap32_sum(ssq); csum = swap32_sum(csum);
    if (lane < 32) {
      *(LAS float*)(lds + T_SQ + (16 + 64 * wm + 32 * wn + lane) * 4) = ssq;
      *(LAS float*)(lds + T_CS + (32 * (2 * wn + wm) + lane) * 4) = csum;
      if (wn == 1) { *(LAS float*)(lds + T_SX + (16 + 64 * wm + lane) * 4) = acc[0][1][12]; *(LAS float*)(lds + T_SX + (16 + 64 * wm + 32 + lane) * 4) = acc[1][1][12]; }
    }
  }
  bar_lgkm();
  if (w < 4) {
    const int wm = w >> 1, wn = w & 1;
#pragma unroll
    for (int mt = 0; mt < 2; ++mt) {
      const int tl = 2 * wm + mt, tt = 16 + 32 * tl + l31;
      const float sx = *(LAS float*)(lds + T_SX + tt * 4), sq = *(LAS float*)(lds + T_SQ + tt * 4);
      const float mu = sx * (1.0f / 640.0f), var = __builtin_fmaxf(sq * (1.0f / 640.0f) - mu * mu, 0.f);
      const float rstd = 1.0f / __builtin_sqrtf(var + 1e-5f);
#pragma unroll
      for (int nt = 0; nt < 2; ++nt)
#pragma unroll
        for (int g = 0; g < 4; ++g) {
          const int t = 2 * wn + nt, qi = 8 * t + 2 * g + h;
          const f32x4 cs = *(LAS f32x4*)(lds + T_CS + (32 * t + 8 * g + 4 * h) * 4);
          const int mat = qi / 10, hh = (qi % 10) / 5, qd = qi % 5, head = 2 * hp + hh;
          const float sc = mat == 0 ? rstd * QSCALE : rstd;
          float v[4];
#pragma unroll
          for (int i = 0; i < 4; ++i) v[i] = sc * (acc[mt][nt][4 * g + i] - mu * cs[i]);
          const u32x2 pk = {pkbf(v[0], v[1]), pkbf(v[2], v[3])};
          if (!BL) {
            const int pos = (qd + 4 * hh) % 5;
            if (qi < 30) {
              unsigned off;
              if (mat == 0) off = qoff + (head * 64 + 4 * rt + tl) * 1536 + (pos >> 1) * 512 + l31 * 16 + (pos & 1) * 8;
              else if (mat == 1) off = kvoff + (head * 16 + rt) * KVSTAGE + KVK + tl * 1536 + (pos >> 1) * 512 + l31 * 16 + (pos & 1) * 8;
              else off = kvoff + (head * 16 + rt) * KVSTAGE + KVV + tl * 1536 + qd * 256 + (((l31 >> 2) ^ qd) * 32) + (l31 & 3) * 8;
              __builtin_amdgcn_raw_buffer_store_b64(pk, rs, off, 0, SC1);
              if (mat == 1 && pos == 4) __builtin_amdgcn_raw_buffer_store_b64((u32x2){0x3f80u, 0u}, rs, off + 8, 0, SC1);
              if (mat == 2 && qd == 4) __builtin_amdgcn_raw_buffer_store_b64((u32x2){0x3f80u, 0u}, rs, kvoff + (head * 16 + rt) * KVSTAGE + KVV + tl * 1536 + 5 * 256 + (((l31 >> 2) ^ 5) * 32) + (l31 & 3) * 8, 0, SC1);
            }
          } else if (qi < 20) *(LAS u32x2*)(lds + L_QK + (qi * 144 + tt) * 8) = pk;
          else if (qi < 30) *(LAS f32x4*)(lds + L_V + ((qi - 20) * 144 + tt) * 16) = (f32x4){v[0], v[1], v[2], v[3]};
        }
    }
  } else if (BL) {
    const int lw = w - 4, g = lane >> 4, tt = lane & 15;
    const float sx = *(LAS float*)(lds + T_SX + tt * 4), sq = *(LAS float*)(lds + T_SQ + tt * 4);
    const float mu = sx * (1.0f / 640.0f), var = __builtin_fmaxf(sq * (1.0f / 640.0f) - mu * mu, 0.f);
    const float rstd = 1.0f / __builtin_sqrtf(var + 1e-5f);
#pragma unroll
    for (int n = 0; n < 2; ++n) {
      const int qi = 4 * (2 * lw + n) + g;
      const f32x4 cs = *(LAS f32x4*)(lds + T_CS + qi * 16);
      float v[4];
#pragma unroll
      for (int i = 0; i < 4; ++i) v[i] = rstd * (hacc[n][i] - mu * cs[i]);
      if (qi >= 10 && qi < 20) *(LAS u32x2*)(lds + L_QK + (qi * 144 + tt) * 8) = (u32x2){pkbf(v[0], v[1]), pkbf(v[2], v[3])};
      else if (qi >= 20 && qi < 30) *(LAS f32x4*)(lds + L_V + ((qi - 20) * 144 + tt) * 16) = (f32x4){v[0], v[1], v[2], v[3]};
    }
  }
  if (BL) {
    bar_lgkm();
    const int hh = w & 1, tg = w >> 1, tt = 16 + 32 * tg + l31, tok = 128 * rt + 32 * tg + l31;
    unsigned q[10];
#pragma unroll
    for (int d = 0; d < 5; ++d) { const u32x2 a = *(LAS u32x2*)(lds + L_QK + ((5 * hh + d) * 144 + tt) * 8); q[2 * d] = a[0]; q[2 * d + 1] = a[1]; }
    float lg[4];
#pragma unroll
    for (int i = 0; i < 4; ++i) {
      const int wi = 4 * h + i; float a = 0.f;
#pragma unroll
      for (int d = 0; d < 5; ++d) { const u32x2 k = *(LAS u32x2*)(lds + L_QK + ((10 + 5 * hh + d) * 144 + tt - 2 * wi) * 8); a = dot2(q[2 * d], k[0], a); a = dot2(q[2 * d + 1], k[1], a); }
      lg[i] = (tok - 2 * wi >= 0) ? a : -INFINITY;
    }
    const float m = swap32_max(__builtin_fmaxf(__builtin_fmaxf(lg[0], lg[1]), __builtin_fmaxf(lg[2], lg[3])));
    float pw[4], l = 0.f;
#pragma unroll
    for (int i = 0; i < 4; ++i) { pw[i] = ex2(lg[i] - m); l += pw[i]; }
    l = swap32_sum(l);
    float o[20];
#pragma unroll
    for (int d = 0; d < 20; ++d) o[d] = 0.f;
#pragma unroll
    for (int i = 0; i < 4; ++i) {
      const int wi = 4 * h + i; const int kt = (tok - 2 * wi >= 0) ? tt - 2 * wi : tt;
#pragma unroll
      for (int d = 0; d < 5; ++d) { const f32x4 v = *(LAS f32x4*)(lds + L_V + ((5 * hh + d) * 144 + kt) * 16);
#pragma unroll
        for (int e = 0; e < 4; ++e) o[4 * d + e] = __builtin_fmaf(pw[i], v[e], o[4 * d + e]); }
    }
    const float inv = 1.0f / l;
#pragma unroll
    for (int d = 0; d < 20; ++d) o[d] = swap32_sum(o[d]) * inv;
    const unsigned ob = ooff + ((4 * rt + tg) * 160 + (2 * hp + hh) * 5) * 256 + l31 * 8;
#pragma unroll
    for (int j = 0; j < 3; ++j) {
      const int d0 = h ? 3 + j : j;
      const float a0 = h ? o[(12 + 4 * j) % 20] : o[4 * j], a1 = h ? o[(13 + 4 * j) % 20] : o[4 * j + 1], a2 = h ? o[(14 + 4 * j) % 20] : o[4 * j + 2], a3 = h ? o[(15 + 4 * j) % 20] : o[4 * j + 3];
      if (d0 < 5) __builtin_amdgcn_raw_buffer_store_b64((u32x2){pkbf(a0, a1), pkbf(a2, a3)}, rs, ob + d0 * 256, 0, SC1);
    }
  }
}

DI void phase_attn(lchar* lds, const Params& p, unsigned qoff, unsigned kvoff, unsigned ooff, int x, int head) {
  const unsigned lds0 = (unsigned)(uintptr_t)lds;
  const int tid = threadIdx.x, lane = tid & 63, w = __builtin_amdgcn_readfirstlane(tid >> 6);
  const int h = lane >> 5, l31 = lane & 31;
  const int qg = w & 3, kh = w >> 2;
  rsrc_t rs = __builtin_amdgcn_make_buffer_rsrc((void*)p.ws, 0, WS_END, 0x00020000);
  const char* kvhead = p.ws + kvoff + (size_t)head * 16 * KVSTAGE + w * 3072;
  const unsigned dvoff = lane * 16;
  auto issue = [&](int r, int slot) { dma3(dvoff, kvhead + ((x + r) & 7) * ROUND, lds0 + slot * ROUND + w * 3072); };
  const int tb = 8 * x + 2 * qg;
  const unsigned qb = qoff + (head * 64 + tb) * 1536;
  bf16x8 qA0 = __builtin_bit_cast(bf16x8, __builtin_amdgcn_raw_buffer_load_b128(rs, qb + h * 512 + l31 * 16, 0, SC1));
  bf16x8 qB0 = __builtin_bit_cast(bf16x8, __builtin_amdgcn_raw_buffer_load_b128(rs, qb + 1536 + h * 512 + l31 * 16, 0, SC1));
  u32x2 qa1 = __builtin_amdgcn_raw_buffer_load_b64(rs, qb + 1024 + l31 * 16, 0, SC1);
  u32x2 qb1 = __builtin_amdgcn_raw_buffer_load_b64(rs, qb + 1536 + 1024 + l31 * 16, 0, SC1);
  issue(0, 0); issue(1, 1);
  const lchar* kp0 = lds + kh * KVSTAGE + KVK + h * 512 + l31 * 16;
  const lchar* kp1 = lds + kh * KVSTAGE + KVK + 1024 + l31 * 16 + h * 8;
  const int dg = (lane >> 4) & 1, qq = (lane & 15) >> 2, p4 = lane & 3;
  const int qd = dg ? (p4 == 0 ? 4 : 5) : p4;
  const lchar* vp00 = lds + kh * KVSTAGE + KVV + qd * 256 + (((0 + h) ^ qd) * 32) + qq * 8;
  const lchar* vp01 = lds + kh * KVSTAGE + KVV + qd * 256 + (((2 + h) ^ qd) * 32) + qq * 8;
  const lchar* vp10 = lds + kh * KVSTAGE + KVV + qd * 256 + (((4 + h) ^ qd) * 32) + qq * 8;
  const lchar* vp11 = lds + kh * KVSTAGE + KVV + qd * 256 + (((6 + h) ^ qd) * 32) + qq * 8;
  asm volatile("s_waitcnt vmcnt(3)\n\ts_barrier" ::: "memory");
  f32x16 oA = {}, oB = {}, sA, sB;
  u32x4 pA0 = {}, pA1 = {}, pB0 = {}, pB1 = {};
  bf16x8 kf0, vf0, vf1; s16x4 kf1;
  const f32x16 zero = {};
  s16x4 qA1, qB1;
  {
    const bf16x8 a0 = lds_b128(lds + KVK + h * 512 + l31 * 16);
    const s16x4 a1 = lds_b64(lds + KVK + 1024 + l31 * 16 + h * 8);
    const u32x2 ta = {h ? 0u : qa1[0], h ? 0u : qa1[1]}, tb2 = {h ? 0u : qb1[0], h ? 0u : qb1[1]};
    f32x16 sa = MFMA32(a0, qA0, zero); sa = MFMA8(a1, __builtin_bit_cast(s16x4, ta), sa);
    f32x16 sb = MFMA32(a0, qB0, zero); sb = MFMA8(a1, __builtin_bit_cast(s16x4, tb2), sb);
    float ma = sa[0], mb = sb[0];
#pragma unroll
    for (int r = 1; r < 16; ++r) { ma = __builtin_fmaxf(ma, sa[r]); mb = __builtin_fmaxf(mb, sb[r]); }
    ma = swap32_max(ma); mb = swap32_max(mb);
    const unsigned na = pkbf(-ma, 0.f), nb = pkbf(-mb, 0.f);
    const u32x2 ua = {h ? na : qa1[0], h ? 0u : qa1[1]}, ub = {h ? nb : qb1[0], h ? 0u : qb1[1]};
    qA1 = __builtin_bit_cast(s16x4, ua); qB1 = __builtin_bit_cast(s16x4, ub);
  }
#define KRD(P0, P1, TL) do { kf0 = lds_b128((P0) + (TL) * 1536); kf1 = lds_b64((P1) + (TL) * 1536); } while (0)
#define VRD(TL) do { vf0 = cat(lds_tr(vp00 + (TL) * 1536), lds_tr(vp01 + (TL) * 1536)); vf1 = cat(lds_tr(vp10 + (TL) * 1536), lds_tr(vp11 + (TL) * 1536)); } while (0)
  KRD(kp0, kp1, 0); VRD(0);
  sA = MFMA32(kf0, qA0, zero); sA = MFMA8(kf1, qA1, sA);
  constexpr int PRIO = 7;
#define SP(Y, g) do { if (Y) { if (((PRIO >> (g)) & 1) != ((PRIO >> (((g) + 3) & 3)) & 1)) __builtin_amdgcn_s_setprio((PRIO >> (g)) & 1); } } while (0)
#define HSTEP(Y, SC, PC0, PC1, SN, QN0, QN1, ON, PN0, PN1, LOADK, LOADV) do { \
    SBAR(); SP(Y, 0); \
    SN = MFMA32(kf0, QN0, zero); SBAR(); \
    SC[0] = ex2(SC[0]); SC[1] = ex2(SC[1]); SC[2] = ex2(SC[2]); SC[3] = ex2(SC[3]); PIN(SC); SBAR(); SP(Y, 1); \
    SN = MFMA8(kf1, QN1, SN); SBAR(); \
    LOADK; \
    SC[4] = ex2(SC[4]); SC[5] = ex2(SC[5]); SC[6] = ex2(SC[6]); SC[7] = ex2(SC[7]); \
    PC0[0] = pkbf(SC[0], SC[1]); PC0[1] = pkbf(SC[2], SC[3]); PIN(SC); PIN(PC0); SBAR(); SP(Y, 2); \
    ON = MFMA32(vf0, __builtin_bit_cast(bf16x8, PN0), ON); SBAR(); \
    SC[8] = ex2(SC[8]); SC[9] = ex2(SC[9]); SC[10] = ex2(SC[10]); SC[11] = ex2(SC[11]); \
    PC0[2] = pkbf(SC[4], SC[5]); PC0[3] = pkbf(SC[6], SC[7]); PIN(SC); PIN(PC0); SBAR(); SP(Y, 3); \
    ON = MFMA32(vf1, __builtin_bit_cast(bf16x8, PN1), ON); SBAR(); \
    LOADV; \
    SC[12] = ex2(SC[12]); SC[13] = ex2(SC[13]); SC[14] = ex2(SC[14]); SC[15] = ex2(SC[15]); \
    PC1[0] = pkbf(SC[8], SC[9]); PC1[1] = pkbf(SC[10], SC[11]); \
    PC1[2] = pkbf(SC[12], SC[13]); PC1[3] = pkbf(SC[14], SC[15]); PIN(PC1); SBAR(); \
  } while (0)
#define NOP_ do {} while (0)
#define XSTEP(Y, LK, LV) HSTEP(Y, sA, pA0, pA1, sB, qB0, qB1, oB, pB0, pB1, LK, LV)
#define YSTEP(Y) HSTEP(Y, sB, pB0, pB1, sA, qA0, qA1, oA, pA0, pA1, NOP_, NOP_)
  int slot = 0;
#define ROUNDS(Y) \
  for (int r = 0; r < 8; ++r) { \
    const int nslot = slot == 2 ? 0 : slot + 1, n2slot = slot == 0 ? 2 : slot - 1; \
    const int dn = (nslot - slot) * ROUND; \
    const lchar* kn0 = kp0 + dn; const lchar* kn1 = kp1 + dn; \
    XSTEP(Y, KRD(kp0, kp1, 1), VRD(0)); YSTEP(Y); \
    if (Y && r > 0) issue(r + 1, nslot); \
    XSTEP(Y, KRD(kp0, kp1, 2), VRD(1)); YSTEP(Y); \
    XSTEP(Y, KRD(kp0, kp1, 3), VRD(2)); YSTEP(Y); \
    asm volatile("s_waitcnt vmcnt(0)\n\ts_barrier" ::: "memory"); \
    if (!Y) issue(r + 2, n2slot); \
    XSTEP(Y, KRD(kn0, kn1, 0), VRD(3)); YSTEP(Y); \
    kp0 = kn0; kp1 = kn1; vp00 += dn; vp01 += dn; vp10 += dn; vp11 += dn; slot = nslot; \
  }
  if (w >= 4) { ROUNDS(true) __builtin_amdgcn_s_setprio(0); } else { ROUNDS(false) }
  oB = MFMA32(vf0, __builtin_bit_cast(bf16x8, pB0), oB);
  oB = MFMA32(vf1, __builtin_bit_cast(bf16x8, pB1), oB);
  asm volatile("s_waitcnt vmcnt(0) lgkmcnt(0)\n\ts_barrier" ::: "memory");
  {
    LAS f32x4* xb = (LAS f32x4*)(lds) + (qg * 2) * 192 + lane;
    if (kh) { xb[0] = (f32x4){oA[0], oA[1], oA[2], oA[3]}; xb[64] = (f32x4){oA[4], oA[5], oA[6], oA[7]}; xb[128] = (f32x4){oA[8], oA[9], oA[10], oA[11]}; }
    else { xb[192] = (f32x4){oB[0], oB[1], oB[2], oB[3]}; xb[192 + 64] = (f32x4){oB[4], oB[5], oB[6], oB[7]}; xb[192 + 128] = (f32x4){oB[8], oB[9], oB[10], oB[11]}; }
    bar_lgkm();
    const int sel = kh ? 192 : 0;
    const f32x4 a = xb[sel], b = xb[sel + 64], c = xb[sel + 128];
    float o[12];
#pragma unroll
    for (int i = 0; i < 4; ++i) { o[i] = (kh ? oB[i] : oA[i]) + a[i]; o[4 + i] = (kh ? oB[4 + i] : oA[4 + i]) + b[i]; o[8 + i] = (kh ? oB[8 + i] : oA[8 + i]) + c[i]; }
    auto rr = __builtin_amdgcn_permlane32_swap(__float_as_uint(o[8]), __float_as_uint(o[8]), false, false);
    const float l = __uint_as_float(rr[1]);
    const float inv = 1.0f / l;
    const unsigned ob = ooff + ((tb + kh) * 160 + head * 5) * 256 + l31 * 8;
    __builtin_amdgcn_raw_buffer_store_b64((u32x2){pkbf(o[0] * inv, o[1] * inv), pkbf(o[2] * inv, o[3] * inv)}, rs, ob + h * 256, 0, SC1);
    __builtin_amdgcn_raw_buffer_store_b64((u32x2){pkbf(o[4] * inv, o[5] * inv), pkbf(o[6] * inv, o[7] * inv)}, rs, ob + (2 + h) * 256, 0, SC1);
    if (h == 0) __builtin_amdgcn_raw_buffer_store_b64((u32x2){pkbf(o[8] * inv, o[9] * inv), pkbf(o[10] * inv, o[11] * inv)}, rs, ob + 4 * 256, 0, SC1);
  }
}

constexpr int P_RED = 102400;
template <bool FIRST, bool LAST>
DI void phase_proj(lchar* lds, const Params& p, const char* W, unsigned ooff, unsigned xin, unsigned xout, int bx, int bj) {
  const unsigned lds0 = (unsigned)(uintptr_t)lds;
  const int tid = threadIdx.x, lane = tid & 63, w = __builtin_amdgcn_readfirstlane(tid >> 6);
  const int rt = 4 * bx + (bj & 3), ct = bj >> 2;
  const int wm = w & 3, wk = w >> 2, tok16 = lane & 15, g = lane >> 4;
  rsrc_t rs = __builtin_amdgcn_make_buffer_rsrc((void*)p.ws, 0, WS_END, 0x00020000);
  {
    int idx = w * 64 + lane, row = idx / 10, ch = idx - row * 10;
#pragma unroll
    for (int i = 0; i < 13; ++i) {
      if (i < 12 || w < 4) dma1(row * 1280 + ch * 16, W + ct * 160, lds0 + (i * 8 + w) * 1024);
      row += 51; ch += 2; if (ch >= 10) { ch -= 10; row += 1; }
    }
  }
  const int token = 64 * rt + 16 * wm + tok16;
  const unsigned tokoff = (token >> 5) * 40960 + (token & 31) * 8;
  u32x2 a[10][2];
#pragma unroll
  for (int s = 0; s < 10; ++s) {
    a[s][0] = __builtin_amdgcn_raw_buffer_load_b64(rs, ooff + tokoff + (8 * (10 * wk + s) + g) * 256, 0, SC1);
    a[s][1] = __builtin_amdgcn_raw_buffer_load_b64(rs, ooff + tokoff + (8 * (10 * wk + s) + 4 + g) * 256, 0, SC1);
  }
  u32x2 res[3];
#pragma unroll
  for (int j = 0; j < 3; ++j) { const int t = wk ? 3 + (j & 1) : j; const int quad = 20 * ct + 4 * t + g;
    if (FIRST) res[j] = *(const u32x2*)(p.x0 + (size_t)token * 1280 + quad * 8);
    else res[j] = __builtin_amdgcn_raw_buffer_load_b64(rs, xin + tokoff + quad * 256, 0, SC1); }
  asm volatile("s_waitcnt vmcnt(23)\n\ts_barrier" ::: "memory");
  f32x4 acc[5];
#pragma unroll
  for (int t = 0; t < 5; ++t) acc[t] = (f32x4){0.f, 0.f, 0.f, 0.f};
  const int q4 = (lane & 15) >> 2, p4 = lane & 3;
  const lchar* wb = lds + (320 * wk + 4 * g + q4) * 160 + p4 * 8;
#pragma unroll
  for (int s = 0; s < 10; ++s) {
    const bf16x8 bf = cat(__builtin_bit_cast(s16x4, a[s][0]), __builtin_bit_cast(s16x4, a[s][1]));
#pragma unroll
    for (int t = 0; t < 5; ++t) acc[t] = MFMA16(cat(lds_tr(wb + s * 5120 + t * 32), lds_tr(wb + s * 5120 + 2560 + t * 32)), bf, acc[t]);
  }
  LAS f32x4* red = (LAS f32x4*)(lds + P_RED) + wm * 320 + lane;
  if (wk) { red[0] = acc[0]; red[64] = acc[1]; red[128] = acc[2]; } else { red[192] = acc[3]; red[256] = acc[4]; }
  bar_lgkm();
#pragma unroll
  for (int j = 0; j < 3; ++j) {
    if (wk && j == 2) break;
    const int t = wk ? 3 + j : j;
    const f32x4 o = red[t * 64];
    const f32x4 me = wk ? (j == 0 ? acc[3] : acc[4]) : acc[j];
    const float v0 = me[0] + o[0] + bflo(res[j][0]), v1 = me[1] + o[1] + bfhi(res[j][0]), v2 = me[2] + o[2] + bflo(res[j][1]), v3 = me[3] + o[3] + bfhi(res[j][1]);
    const u32x2 pk = {pkbf(v0, v1), pkbf(v2, v3)};
    const int quad = 20 * ct + 4 * t + g;
    if (LAST) *(u32x2*)(p.out + (size_t)token * 1280 + quad * 8) = pk;
    else __builtin_amdgcn_raw_buffer_store_b64(pk, rs, xout + tokoff + quad * 256, 0, SC1);
  }
}

template <int PH> __global__ void __launch_bounds__(512, 2) k_phase(Params p) {
  __shared__ __attribute__((aligned(16))) char smem[163840];
  lchar* lds = (lchar*)smem;
  const int bx = blockIdx.x & 7, bj = blockIdx.x >> 3;
  if (PH == 0) phase_qkv<false, true>(lds, p, p.wqA, 0, WS_Q, WS_KV, 0, bx, bj);
  if (PH == 1) phase_attn(lds, p, WS_Q, WS_KV, WS_O, bx, bj);
  if (PH == 2) phase_proj<true, false>(lds, p, p.woA, WS_O, 0, WS_X, bx, bj);
  if (PH == 3) phase_qkv<true, false>(lds, p, p.wqB, WS_X, 0, 0, WS_O + XB, bx, bj);
  if (PH == 4) phase_proj<false, false>(lds, p, p.woB, WS_O + XB, WS_X, WS_X + XB, bx, bj);
  if (PH == 5) phase_qkv<false, false>(lds, p, p.wqA, WS_X + XB, WS_Q + QBYTES, WS_KV + KVBYTES, 0, bx, bj);
  if (PH == 6) phase_attn(lds, p, WS_Q + QBYTES, WS_KV + KVBYTES, WS_O + 2 * XB, bx, bj);
  if (PH == 7) phase_proj<false, false>(lds, p, p.woA, WS_O + 2 * XB, WS_X + XB, WS_X + 2 * XB, bx, bj);
  if (PH == 8) phase_qkv<true, false>(lds, p, p.wqB, WS_X + 2 * XB, 0, 0, WS_O + 3 * XB, bx, bj);
  if (PH == 9) phase_proj<false, true>(lds, p, p.woB, WS_O + 3 * XB, WS_X + 2 * XB, 0, bx, bj);
}

extern "C" void kernel_launch(void* const* d_in, const int* in_sizes, int n_in, void* d_out, int out_size, void* d_ws, size_t ws_size, hipStream_t stream) {
  Params p;
  p.x0 = (const char*)d_in[0]; p.wqA = (const char*)d_in[1]; p.woA = (const char*)d_in[2];
  p.wqB = (const char*)d_in[4]; p.woB = (const char*)d_in[5];
  p.out = (char*)d_out; p.ws = (char*)d_ws;
  k_phase<0><<<256, 512, 0, stream>>>(p); k_phase<1><<<256, 512, 0, stream>>>(p);
  k_phase<2><<<256, 512, 0, stream>>>(p); k_phase<3><<<256, 512, 0, stream>>>(p);
  k_phase<4><<<256, 512, 0, stream>>>(p); k_phase<5><<<256, 512, 0, stream>>>(p);
  k_phase<6><<<256, 512, 0, stream>>>(p); k_phase<7><<<256, 512, 0, stream>>>(p);
  k_phase<8><<<256, 512, 0, stream>>>(p); k_phase<9><<<256, 512, 0, stream>>>(p);
}
```

```cpp
#include <hip/hip_runtime.h>
#include <stdint.h>

#define DI __device__ __forceinline__
typedef short bf16x8 __attribute__((ext_vector_type(8)));
typedef short s16x4 __attribute__((ext_vector_type(4)));
typedef short v4i16_t __attribute__((ext_vector_type(4)));
typedef float f32x16 __attribute__((ext_vector_type(16)));
typedef float f32x4 __attribute__((ext_vector_type(4)));
typedef unsigned u32x2 __attribute__((ext_vector_type(2)));
typedef unsigned u32x4 __attribute__((ext_vector_type(4)));
typedef unsigned long long u64;
typedef __bf16 bf16x2_t __attribute__((ext_vector_type(2)));
#define LAS __attribute__((address_space(3)))
#define GAS __attribute__((address_space(1)))
typedef LAS char lchar;
typedef __amdgpu_buffer_rsrc_t rsrc_t;
#define SBAR() __builtin_amdgcn_sched_barrier(0)
#define PIN(x) asm volatile("" : "+v"(x))
#define MFMA32(a, b, c) __builtin_amdgcn_mfma_f32_32x32x16_bf16(a, b, c, 0, 0, 0)
#define MFMA8(a, b, c) __builtin_amdgcn_mfma_f32_32x32x8bf16_1k(a, b, c, 0, 0, 0)
#define MFMA16(a, b, c) __builtin_amdgcn_mfma_f32_16x16x32_bf16(a, b, c, 0, 0, 0)
#define SC1 16
#define QABL 0
#define MULTI 0

DI unsigned pkbf(float a, float b) { bf16x2_t v = {(__bf16)a, (__bf16)b}; return __builtin_bit_cast(unsigned, v); }
DI float bflo(unsigned u) { return __uint_as_float(u << 16); }
DI float bfhi(unsigned u) { return __uint_as_float(u & 0xffff0000u); }
DI float ex2(float x) { return __builtin_amdgcn_exp2f(x); }
DI float dot2(unsigned a, unsigned b, float c) { return __builtin_amdgcn_fdot2_f32_bf16(__builtin_bit_cast(bf16x2_t, a), __builtin_bit_cast(bf16x2_t, b), c, false); }
DI void sq8(const bf16x8& f, float& s0, float& s1) {
  const u32x4 u = __builtin_bit_cast(u32x4, f);
#pragma unroll
  for (int i = 0; i < 4; ++i) { const float lo = bflo(u[i]), hi = bfhi(u[i]); s0 = __builtin_fmaf(lo, lo, s0); s1 = __builtin_fmaf(hi, hi, s1); }
}
DI bf16x8 lds_b128(const lchar* p) { return *(const LAS bf16x8*)p; }
DI s16x4 lds_b64(const lchar* p) { return *(const LAS s16x4*)p; }
DI s16x4 lds_tr(const lchar* p) { return __builtin_bit_cast(s16x4, __builtin_amdgcn_ds_read_tr16_b64_v4i16((LAS v4i16_t*)p)); }
DI bf16x8 cat(s16x4 a, s16x4 b) { return __builtin_shufflevector(a, b, 0, 1, 2, 3, 4, 5, 6, 7); }
DI float swap32_sum(float v) { auto r = __builtin_amdgcn_permlane32_swap(__float_as_uint(v), __float_as_uint(v), false, false); return __uint_as_float(r[0]) + __uint_as_float(r[1]); }
DI float swap32_max(float v) { auto r = __builtin_amdgcn_permlane32_swap(__float_as_uint(v), __float_as_uint(v), false, false); return __builtin_fmaxf(__uint_as_float(r[0]), __uint_as_float(r[1])); }
DI float swap16_sum(float v) { auto r = __builtin_amdgcn_permlane16_swap(__float_as_uint(v), __float_as_uint(v), false, false); return __uint_as_float(r[0]) + __uint_as_float(r[1]); }
DI float swap16_max(float v) { auto r = __builtin_amdgcn_permlane16_swap(__float_as_uint(v), __float_as_uint(v), false, false); return __builtin_fmaxf(__uint_as_float(r[0]), __uint_as_float(r[1])); }

constexpr int S = 2048, D = 640;
constexpr float QSCALE = 0.22360679774997896f * 1.4426950408889634f;
constexpr unsigned XB = S * D * 2;
constexpr int KVSTAGE = 12288;
constexpr int KVK = 0, KVV = 6144, ROUND = 2 * KVSTAGE;
constexpr unsigned QBYTES = 32 * 64 * 1536, KVBYTES = 32 * 16 * KVSTAGE;
constexpr unsigned PWB = 16 * 10 * 16384, POB_ = 8 * 102400;
constexpr unsigned WS_FLAG = 0;
constexpr unsigned WS_NONCE = 10 * 256 * 8;
constexpr unsigned WS_X = 32768;
constexpr unsigned WS_O = WS_X + 3 * XB;
constexpr unsigned WS_Q = WS_O + 4 * XB;
constexpr unsigned WS_KV = WS_Q + 2 * QBYTES;
constexpr unsigned WS_PW = WS_KV + 2 * KVBYTES;
constexpr unsigned WS_PO = WS_PW + 2 * PWB;
constexpr unsigned WS_CS = WS_PO + 2 * POB_;
constexpr unsigned WS_SQ = WS_CS + 256 * 512;
constexpr unsigned SQB = S * 64;
constexpr unsigned WS_END = WS_SQ + 3 * SQB;

struct Params { const char* x0; const char* wqA; const char* woA; const char* wqB; const char* woB; char* out; char* ws; };

#define TS(i) do {} while (0)
#define TSDECL do {} while (0)
#define TSOUT(ph) do {} while (0)

DI const char* uni(const char* p) { const uint64_t v = (uint64_t)p; const unsigned lo = __builtin_amdgcn_readfirstlane((unsigned)v), hi = __builtin_amdgcn_readfirstlane((unsigned)(v >> 32)); return (const char*)(((uint64_t)hi << 32) | lo); }
DI unsigned uni(unsigned v) { return __builtin_amdgcn_readfirstlane(v); }
#define GLDS "global_load_lds_dwordx4 "
DI void dma1(unsigned v, const char* sb, unsigned dst) {
  asm volatile("s_mov_b32 m0, %2\n\ts_nop 3\n\t" GLDS "%0, %1 sc1" :: "v"(v), "s"(uni(sb)), "s"(uni(dst)) : "memory"); }
DI void dma1h(unsigned v, const char* sb, unsigned dst) {
  asm volatile("s_mov_b32 m0, %2\n\ts_mov_b64 exec, 0xffffffff\n\ts_nop 2\n\t" GLDS "%0, %1 sc1\n\ts_mov_b64 exec, -1" :: "v"(v), "s"(uni(sb)), "s"(uni(dst)) : "memory"); }
DI void dma3(unsigned v, const char* sb, unsigned dst) {
  asm volatile("s_mov_b32 m0, %2\n\ts_nop 3\n\t" GLDS "%0, %1 sc1\n\t" GLDS "%0, %1 offset:1024 sc1\n\t" GLDS "%0, %1 offset:2048 sc1" :: "v"(v), "s"(uni(sb)), "s"(uni(dst)) : "memory"); }
DI void dma4(unsigned v, const char* sb, unsigned dst) {
  asm volatile("s_mov_b32 m0, %2\n\ts_nop 3\n\t" GLDS "%0, %1 sc1\n\t" GLDS "%0, %1 offset:1024 sc1\n\t" GLDS "%0, %1 offset:2048 sc1\n\t" GLDS "%0, %1 offset:3072 sc1"
               :: "v"(v), "s"(uni(sb)), "s"(uni(dst)) : "memory"); }
DI void dma4v(unsigned v0, unsigned v1, unsigned v2, unsigned v3, const char* sb, unsigned dst) {
  asm volatile("s_mov_b32 m0, %5\n\ts_nop 3\n\t" GLDS "%0, %4\n\t" GLDS "%1, %4 offset:1024\n\t" GLDS "%2, %4 offset:2048\n\t" GLDS "%3, %4 offset:3072"
               :: "v"(v0), "v"(v1), "v"(v2), "v"(v3), "s"(uni(sb)), "s"(uni(dst)) : "memory"); }
DI void dma4vm(unsigned v0, unsigned v1, unsigned v2, unsigned v3, const char* sb, unsigned dst, u64 m0_, u64 m1, u64 m2, u64 m3) {
  asm volatile("s_mov_b32 m0, %5\n\ts_mov_b64 exec, %6\n\ts_nop 2\n\t" GLDS "%0, %4\n\ts_mov_b64 exec, %7\n\t" GLDS "%1, %4 offset:1024\n\ts_mov_b64 exec, %8\n\t" GLDS "%2, %4 offset:2048\n\t"
               "s_mov_b64 exec, %9\n\t" GLDS "%3, %4 offset:3072\n\ts_mov_b64 exec, -1"
               :: "v"(v0), "v"(v1), "v"(v2), "v"(v3), "s"(uni(sb)), "s"(uni(dst)), "s"(m0_), "s"(m1), "s"(m2), "s"(m3) : "memory"); }
template <int N> DI void wait_vm() { asm volatile("s_waitcnt vmcnt(%0)" :: "i"(N) : "memory"); }
DI void bar() { asm volatile("s_barrier" ::: "memory"); }
DI void bar_lgkm() { asm volatile("s_waitcnt lgkmcnt(0)\n\ts_barrier" ::: "memory"); }
DI void bar_vm_lgkm() { asm volatile("s_waitcnt vmcnt(0) lgkmcnt(0)\n\ts_barrier" ::: "memory"); }

struct Sync { const char* ws; u64 tag0; };
DI void flag_set(const Sync& sy, int ph, int idx) {
  if (!MULTI) __hip_atomic_store((GAS u64*)(sy.ws + WS_FLAG + (ph * 256 + idx) * 8), sy.tag0 + ph, __ATOMIC_RELAXED, __HIP_MEMORY_SCOPE_AGENT);
}
DI void flag_wait(const Sync& sy, int ph, int idx, int n, int lane) {
  if (MULTI) return;
  const GAS u64* f = (const GAS u64*)(sy.ws + WS_FLAG + (ph * 256 + idx + (lane < n ? lane : n - 1)) * 8);
  const u64 want = sy.tag0 + ph;
  const u64 t0 = __builtin_amdgcn_s_memrealtime();
  for (;;) {
    const u64 v = __hip_atomic_load(f, __ATOMIC_RELAXED, __HIP_MEMORY_SCOPE_AGENT);
    if (__all(v == want)) break;
    if (__builtin_amdgcn_s_memrealtime() - t0 > 2000000ull) break;
    __builtin_amdgcn_s_sleep(1);
  }
  asm volatile("" ::: "memory");
}

constexpr int QSLOT = 34816, QA = 0, QW = 16384, QH = 32768;
constexpr int T_SQ = 162048, T_SX = T_SQ + 640, T_CS = T_SX + 640;
constexpr int IMG = 104448;
constexpr int IA_Q = IMG, IA_KV = IMG + 12288, I_DUMMY = IMG + 49152;
constexpr int IB_Q = IMG, IB_K = IMG + 15360, IB_V = IMG + 30720;

DI void proj_w_issue(lchar* lds, const char* pw, int ct, int w, int lane, int g0, int g1) {
  const unsigned lds0 = (unsigned)(uintptr_t)lds;
  for (int g = g0 + w; g < g1; g += 8) dma4(lane * 16, pw + (size_t)ct * 102400 + g * 4096, lds0 + g * 4096);
}

template <bool BL, bool FIRST>
DI void qkv_issueW(lchar* lds, const char* W, int hp, int lw, int lane, int t, const unsigned (&wv)[4], const u64 (&wmk)[4]) {
  const unsigned lds0 = (unsigned)(uintptr_t)lds;
  if (FIRST) dma4vm(wv[0], wv[1], wv[2], wv[3], W + (size_t)(64 * t + 16 * lw) * 3840, lds0 + (t & 3) * QSLOT + QW + lw * 4096, wmk[0], wmk[1], wmk[2], wmk[3]);
  else dma4(lane * 16, W + (size_t)(hp * 10 + t) * 16384 + lw * 4096, lds0 + (t & 3) * QSLOT + QW + lw * 4096);
}
DI void qkv_prefetch(lchar* lds, const char* PW, int bj) {
  const int lane = threadIdx.x & 63, w = __builtin_amdgcn_readfirstlane(threadIdx.x >> 6);
  const unsigned lds0 = (unsigned)(uintptr_t)lds;
  if (w >= 4) {
#pragma unroll
    for (int t = 0; t < 4; ++t) dma4(lane * 16, PW + (size_t)((bj >> 1) * 10 + t) * 16384 + (w - 4) * 4096, lds0 + t * QSLOT + QW + (w - 4) * 4096);
  }
}

template <bool BL, bool FIRST, int PH>
DI void phase_qkv(lchar* lds, const Params& p, const Sync& sy, const char* W, unsigned xoff, unsigned sqoff, unsigned qoff, unsigned kvoff, unsigned ooff, const char* nextw, int bx, int bj) {
  TSDECL; TS(0);
  const int tid = threadIdx.x, lane = tid & 63, w = __builtin_amdgcn_readfirstlane(tid >> 6);
  const unsigned lds0 = (unsigned)(uintptr_t)lds;
  const int rt = 2 * bx + (bj & 1), hp = bj >> 1;
  const int h = lane >> 5, l31 = lane & 31;
  rsrc_t rs = __builtin_amdgcn_make_buffer_rsrc((void*)p.ws, 0, WS_END, 0x00020000);
  constexpr int NA = BL ? 5 : 4, NW = 4;
  f32x16 acc[2][2];
  f32x4 hacc[2];
  float sqv[2] = {0.f, 0.f};
  u32x4 pk[7];
  if (w >= 4) {
    const int lw = w - 4;
    unsigned wv[4] = {0, 0, 0, 0}; u64 wmk[4] = {0, 0, 0, 0}; unsigned av[4] = {0, 0, 0, 0};
    if (FIRST) {
#pragma unroll
      for (int i = 0; i < 4; ++i) { const int q = lane >> 4, c = (lane & 15) ^ ((q << 2) | i), cc = c == 15 ? 14 : c;
        wv[i] = (q + 4 * i) * 3840 + (cc / 5) * 1280 + 80 * hp + (cc % 5) * 16 - 1024 * i; wmk[i] = __ballot(c != 15);
        av[i] = ((lane >> 3) + 8 * i) * 1280 + (((lane & 7) ^ ((4 * i + (lane >> 4)) & 7)) * 16) - 1024 * i; }
    }
    const int htb = rt == 0 ? 0 : 4 * rt - 1;
    const unsigned hv = (lane >> 4) * 512 + 256 + (lane & 15) * 16;
    auto issueA = [&](int t) {
      if (FIRST) dma4v(av[0], av[1], av[2], av[3], p.x0 + (size_t)(128 * rt + 32 * lw) * 1280 + t * 128, lds0 + (t & 3) * QSLOT + QA + lw * 4096);
      else dma4(lane * 16, p.ws + xoff + (size_t)(4 * rt + lw) * 40960 + t * 4096, lds0 + (t & 3) * QSLOT + QA + lw * 4096);
    };
    auto issueH = [&](int t) { if (BL) dma1h(hv, p.ws + xoff + (size_t)htb * 40960 + (8 * t + 2 * lw) * 512, lds0 + (t & 3) * QSLOT + QH + lw * 512); };
    if (FIRST || MULTI) {
#pragma unroll
      for (int t = 0; t < 4; ++t) qkv_issueW<BL, FIRST>(lds, W, hp, lw, lane, t, wv, wmk);
    }
    u32x4 csr = {0u, 0u, 0u, 0u};
    if (!BL && !FIRST) csr = __builtin_amdgcn_raw_buffer_load_b128(rs, WS_CS + blockIdx.x * 512 + (8 * lw + (lane & 7)) * 16, 0, SC1);
    if (!FIRST) flag_wait(sy, PH - 1, 16 * rt, 16, lane);
    issueA(0); issueA(1); issueA(2); issueA(3);
    if (BL) { if (rt > 0) flag_wait(sy, PH - 1, 16 * rt - 8, 8, lane); issueH(0); issueH(1); issueH(2); issueH(3); }
    TS(1);
    const int g = lane >> 4, q4 = (lane & 15) >> 2, p4 = lane & 3;
    const lchar* hx = lds + QH + g * 256 + (lane & 15) * 16;
    const lchar* hw[2][2];
#pragma unroll
    for (int n = 0; n < 2; ++n)
#pragma unroll
      for (int tt = 0; tt < 2; ++tt) { const int row = 8 * g + 4 * tt + q4, ch = 2 * (2 * lw + n) + (p4 >> 1);
        hw[n][tt] = lds + QW + row * 256 + (ch ^ (((row & 3) << 2) | ((row >> 2) & 3))) * 16 + 8 * (p4 & 1); }
    const lchar* sx_[4];
#pragma unroll
    for (int ks = 0; ks < 4; ++ks) sx_[ks] = lds + QA + (32 * lw + l31) * 128 + (((2 * ks + h) ^ ((l31 >> 1) & 7)) * 16);
    hacc[0] = (f32x4){0.f, 0.f, 0.f, 0.f}; hacc[1] = hacc[0];
    float sq0 = 0.f, sq1 = 0.f;
    const bf16x8 ones8 = {0x3f80, 0x3f80, 0x3f80, 0x3f80, 0x3f80, 0x3f80, 0x3f80, 0x3f80};
    constexpr bool CSUM = BL || FIRST;
    u32x4 hsq = {0u, 0u, 0u, 0u};
    if (BL) hsq = __builtin_amdgcn_raw_buffer_load_b128(rs, sqoff + (32 * htb + 16 + (lane & 15)) * 64 + g * 16, 0, SC1);
#define LSTAGE(T, N) do { wait_vm<N>(); bar_lgkm(); \
      const int so = ((T) & 3) * QSLOT; \
      bf16x8 hxf[2], hwf[2][2]; \
      if (CSUM) { _Pragma("unroll") for (int s = 0; s < 2; ++s) { \
        if (BL) hxf[s] = lds_b128(hx + so + s * 1024); else hxf[s] = ones8; \
        _Pragma("unroll") for (int n = 0; n < 2; ++n) hwf[s][n] = cat(lds_tr(hw[n][0] + so + s * 8192), lds_tr(hw[n][1] + so + s * 8192)); } } \
      SBAR(); \
      if (QABL != 1 && (T) >= 2 && (T) + 2 <= 9) { issueA((T) + 2); issueH((T) + 2); qkv_issueW<BL, FIRST>(lds, W, hp, lw, lane, (T) + 2, wv, wmk); } \
      if (CSUM) { _Pragma("unroll") for (int s = 0; s < 2; ++s) { if (BL && (lane & 15) == 0) hxf[s] = ones8; \
        _Pragma("unroll") for (int n = 0; n < 2; ++n) hacc[n] = MFMA16(hwf[s][n], hxf[s], hacc[n]); } } \
      if (FIRST) { _Pragma("unroll") for (int ks = 0; ks < 4; ++ks) sq8(lds_b128(sx_[ks] + so), sq0, sq1); } } while (0)
    constexpr int SS = NA + NW;
    LSTAGE(0, BL ? 3 : 12); TS(2); LSTAGE(1, BL ? 2 : 8); LSTAGE(2, BL ? 1 : 4); LSTAGE(3, SS); LSTAGE(4, SS);
    LSTAGE(5, SS); LSTAGE(6, SS); LSTAGE(7, SS); LSTAGE(8, SS); LSTAGE(9, 0); TS(3);
    if (FIRST) { const float q_ = swap32_sum(sq0 + sq1); if (lane < 32) *(LAS float*)(lds + T_SQ + (16 + 32 * lw + lane) * 4) = q_; }
    if (CSUM) { if ((lane & 15) == 0) { *(LAS f32x4*)(lds + T_CS + (4 * (2 * lw) + g) * 16) = hacc[0]; *(LAS f32x4*)(lds + T_CS + (4 * (2 * lw + 1) + g) * 16) = hacc[1]; } }
    if (FIRST) { if ((lane & 15) == 0) {
        __builtin_amdgcn_raw_buffer_store_b128(__builtin_bit_cast(u32x4, hacc[0]), rs, WS_CS + blockIdx.x * 512 + (4 * (2 * lw) + g) * 16, 0, SC1);
        __builtin_amdgcn_raw_buffer_store_b128(__builtin_bit_cast(u32x4, hacc[1]), rs, WS_CS + blockIdx.x * 512 + (4 * (2 * lw + 1) + g) * 16, 0, SC1); } }
    if (!CSUM) { if (lane < 8) *(LAS u32x4*)(lds + T_CS + (8 * lw + lane) * 16) = csr; }
    if (BL) {
      const f32x4 hf = __builtin_bit_cast(f32x4, hsq);
      sqv[0] = swap32_sum(swap16_sum((hf[0] + hf[1]) + (hf[2] + hf[3])));
      if (lw == 3 && lane >= 32 && lane < 48) *(LAS float*)(lds + T_SX + (lane - 32) * 4) = hacc[1][0];
    }
  } else {
    const int wm = w >> 1, wn = w & 1;
    if (FIRST) {
      { const int sl = w, row = lane; const int pc = 15 ^ (((row & 3) << 2) | ((row >> 2) & 3));
        *(LAS u32x4*)(lds + sl * QSLOT + QW + row * 256 + pc * 16) = (u32x4){0x3f803f80u, 0x3f803f80u, 0x3f803f80u, 0x3f803f80u}; }
      const int gw = blockIdx.x * 4 + w;
#pragma unroll
      for (int k = 0; k < 7; ++k) {
        const int u = gw + 1024 * k; const char* src;
        if (u < 5120) { const int m = u >= 2560, r = u - 2560 * m, hq = r / 160, t = (r % 160) >> 4, rg = r & 15;
          const int q = lane >> 4, c = (lane & 15) ^ ((q << 2) | (rg & 3)), cc = c == 15 ? 14 : c;
          src = (m ? p.wqB : p.wqA) + (size_t)(64 * t + 4 * rg + q) * 3840 + (cc / 5) * 1280 + 80 * hq + (cc % 5) * 16;
        } else { const int v = (u < 6720 ? u : 5120) - 5120, m = v >= 800, r = v - 800 * m, ct = r / 100, idx = 64 * (r % 100) + lane, pr = idx / 10, ch = idx - 10 * pr;
          const int row = (pr & ~12) | ((pr & 4) << 1) | ((pr & 8) >> 1);
          src = (m ? p.woB : p.woA) + (size_t)row * 1280 + ct * 160 + ch * 16; }
        pk[k] = *(const u32x4*)src;
      }
    }
    const lchar* xa[2][4];
#pragma unroll
    for (int a = 0; a < 2; ++a) { const int mt = a ? 1 - wn : wn;
#pragma unroll
      for (int ks = 0; ks < 4; ++ks)
        xa[a][ks] = FIRST ? lds + QA + (64 * wm + 32 * mt + l31) * 128 + (((2 * ks + h) ^ ((l31 >> 1) & 7)) * 16)
                          : lds + QA + (2 * wm + mt) * 4096 + h * 512 + l31 * 16; }
    const int blk = (lane >> 4) & 1, q4 = (lane & 15) >> 2, p4 = lane & 3;
    const lchar* wa[2][2];
#pragma unroll
    for (int b = 0; b < 2; ++b) { const int nt = b ? 1 - wm : wm;
#pragma unroll
      for (int tt = 0; tt < 2; ++tt) { const int row = 8 * h + 4 * tt + q4, ch = 4 * (2 * wn + nt) + 2 * blk + (p4 >> 1);
        wa[b][tt] = lds + QW + row * 256 + (ch ^ (((row & 3) << 2) | ((row >> 2) & 3))) * 16 + 8 * (p4 & 1); } }
#pragma unroll
    for (int a = 0; a < 2; ++a)
#pragma unroll
      for (int b = 0; b < 2; ++b) acc[a][b] = (f32x16){};
    bf16x8 xf[3][2], wf[3][2];
#define QSCHED 1
#define XRD(BUF, T, KS, a) xf[BUF][a] = FIRST ? lds_b128(xa[a][KS] + ((T) & 3) * QSLOT) : lds_b128(xa[a][0] + ((T) & 3) * QSLOT + (KS) * 1024)
#define WRD(BUF, T, KS, b) wf[BUF][b] = cat(lds_tr(wa[b][0] + ((T) & 3) * QSLOT + (KS) * 4096), lds_tr(wa[b][1] + ((T) & 3) * QSLOT + (KS) * 4096))
#define CLOAD(BUF, T, KS) do { XRD(BUF, T, KS, 0); XRD(BUF, T, KS, 1); WRD(BUF, T, KS, 0); WRD(BUF, T, KS, 1); } while (0)
#define KSTEP(C, N, LD, T, KS) do { \
      acc[0][0] = MFMA32(wf[C][0], xf[C][0], acc[0][0]); SBAR(); \
      if (LD) { XRD(N, T, KS, 0); WRD(N, T, KS, 0); } SBAR(); \
      acc[0][1] = MFMA32(wf[C][1], xf[C][0], acc[0][1]); SBAR(); \
      if (LD) { WRD(N, T, KS, 1); } SBAR(); \
      acc[1][0] = MFMA32(wf[C][0], xf[C][1], acc[1][0]); SBAR(); \
      if (LD) { XRD(N, T, KS, 1); } SBAR(); \
      acc[1][1] = MFMA32(wf[C][1], xf[C][1], acc[1][1]); SBAR(); } while (0)
    TS(1);
    bar_lgkm();
    TS(2);
    u32x4 sqp[2][2];
    if (!FIRST) {
#pragma unroll
      for (int a = 0; a < 2; ++a) { const int tok = 128 * rt + 32 * (2 * wm + (a ? 1 - wn : wn)) + l31;
        sqp[a][0] = __builtin_amdgcn_raw_buffer_load_b128(rs, sqoff + tok * 64 + h * 32, 0, SC1); sqp[a][1] = __builtin_amdgcn_raw_buffer_load_b128(rs, sqoff + tok * 64 + h * 32 + 16, 0, SC1); }
    }
    CLOAD(0, 0, 0); CLOAD(1, 0, 1); SBAR();
#define KS_(I) KSTEP((I) % 3, ((I) + 2) % 3, (I) + 2 < 40, ((I) + 2) >> 2, ((I) + 2) & 3)
#define CSTAGE(T) do { if (QABL == 2) { if ((T) < 9) bar(); break; } \
      KS_(4 * (T)); KS_(4 * (T) + 1); if ((T) < 9) bar(); KS_(4 * (T) + 2); KS_(4 * (T) + 3); } while (0)
    CSTAGE(0); CSTAGE(1); CSTAGE(2); CSTAGE(3); CSTAGE(4); CSTAGE(5); CSTAGE(6); CSTAGE(7); CSTAGE(8); CSTAGE(9);
    TS(3);
    if (!FIRST) {
#pragma unroll
      for (int a = 0; a < 2; ++a) { const f32x4 u0 = __builtin_bit_cast(f32x4, sqp[a][0]), u1 = __builtin_bit_cast(f32x4, sqp[a][1]);
        sqv[a] = swap32_sum(((u0[0] + u0[1]) + (u0[2] + u0[3])) + ((u1[0] + u1[1]) + (u1[2] + u1[3]))); }
    } else {
      const int gw = blockIdx.x * 4 + w;
#pragma unroll
      for (int k = 0; k < 7; ++k) { const int u = gw + 1024 * k;
        if (u < 5120) { const int rg = u & 15, c = (lane & 15) ^ (((lane >> 4) << 2) | (rg & 3)); if (c == 15) pk[k] = (u32x4){0x3f803f80u, 0x3f803f80u, 0x3f803f80u, 0x3f803f80u}; }
        if (u < 6720) __builtin_amdgcn_raw_buffer_store_b128(pk[k], rs, (u < 5120 ? WS_PW + u * 1024 : WS_PO + (u - 5120) * 1024) + lane * 16, 0, SC1); }
    }
    if (lane < 32 && wn == 1) {
      *(LAS float*)(lds + T_SX + (16 + 64 * wm + 32 + lane) * 4) = wm ? acc[0][0][12] : acc[0][1][12];
      *(LAS float*)(lds + T_SX + (16 + 64 * wm + lane) * 4) = wm ? acc[1][0][12] : acc[1][1][12];
    }
  }
  bar_lgkm();
  TS(4);
  if (nextw) {
    if (FIRST) {} else proj_w_issue(lds, nextw, bj >> 2, w, lane, 0, 25);
  }
  if (w < 4) {
    const int wm = w >> 1, wn = w & 1;
    f32x4 cs[2][4]; float mu[2], rstd[2];
#pragma unroll
    for (int b = 0; b < 2; ++b)
#pragma unroll
      for (int g = 0; g < 4; ++g) cs[b][g] = *(LAS f32x4*)(lds + T_CS + (8 * (2 * wn + (b ? 1 - wm : wm)) + 2 * g + h) * 16);
#pragma unroll
    for (int a = 0; a < 2; ++a) { const int tt = 16 + 32 * (2 * wm + (a ? 1 - wn : wn)) + l31;
      const float sx = *(LAS float*)(lds + T_SX + tt * 4), sq = FIRST ? *(LAS float*)(lds + T_SQ + tt * 4) : sqv[a];
      mu[a] = sx * (1.0f / 640.0f);
      rstd[a] = __builtin_amdgcn_rsqf(__builtin_fmaxf(sq * (1.0f / 640.0f) - mu[a] * mu[a], 0.f) + 1e-5f); }
    SBAR();
#pragma unroll
    for (int b = 0; b < 2; ++b)
#pragma unroll
      for (int g = 0; g < 4; ++g) {
        const int qi = 8 * (2 * wn + (b ? 1 - wm : wm)) + 2 * g + h;
        const int mat = (qi >= 10) + (qi >= 20), rem = qi - 10 * mat, hh = rem >= 5, qd = rem - 5 * hh;
        const int pos = hh ? (qd == 0 ? 4 : qd - 1) : qd;
        const float sc = mat == 0 ? QSCALE : 1.0f;
#pragma unroll
        for (int a = 0; a < 2; ++a) {
          const int tl = 2 * wm + (a ? 1 - wn : wn), tt = 16 + 32 * tl + l31;
          const float r = rstd[a] * sc, rm = -r * mu[a];
          const u32x2 o2 = {pkbf(__builtin_fmaf(r, acc[a][b][4 * g], rm * cs[b][g][0]), __builtin_fmaf(r, acc[a][b][4 * g + 1], rm * cs[b][g][1])),
                            pkbf(__builtin_fmaf(r, acc[a][b][4 * g + 2], rm * cs[b][g][2]), __builtin_fmaf(r, acc[a][b][4 * g + 3], rm * cs[b][g][3]))};
          int off;
          if (!BL) {
            const int qk = (mat ? IA_KV + hh * 12288 : IA_Q + hh * 6144) + (pos >> 1) * 512 + l31 * 16 + (pos & 1) * 8;
            const int vv = IA_KV + hh * 12288 + KVV + pos * 256 + (((l31 >> 2) ^ pos) * 32) + (l31 & 3) * 8;
            off = (mat == 2 ? vv : qk) + tl * 1536;
          } else {
            const int par = tt & 1, idx = tt >> 1;
            const int qk = (mat ? IB_K : IB_Q) + (hh * 2 + par) * 3840 + (qd >> 1) * 1280 + idx * 16 + (qd & 1) * 8;
            const int vv = IB_V + (hh * 2 + par) * 4224 + pos * 704 + idx * 8;
            off = mat == 2 ? vv : qk;
          }
          if (qi >= 30) off = I_DUMMY + lane * 8;
          *(LAS u32x2*)(lds + off) = o2;
        }
      }
  } else {
    const int lw = w - 4;
    if (!BL) {
      const int hh = lw >> 1;
#pragma unroll
      for (int i = 0; i < 2; ++i) { const int tl = 2 * (lw & 1) + i;
        if (h == 0) *(LAS u32x2*)(lds + IA_KV + hh * 12288 + tl * 1536 + 1024 + l31 * 16 + 8) = (u32x2){0x3f80u, 0u};
        else *(LAS u32x2*)(lds + IA_KV + hh * 12288 + KVV + tl * 1536 + 5 * 256 + l31 * 8) = (u32x2){0x3f80u, 0u}; }
    } else {
      const int g = lane >> 4, tt = lane & 15;
      const float sx = *(LAS float*)(lds + T_SX + tt * 4);
      const float mu = sx * (1.0f / 640.0f);
      const float rstd = __builtin_amdgcn_rsqf(__builtin_fmaxf(sqv[0] * (1.0f / 640.0f) - mu * mu, 0.f) + 1e-5f);
      const f32x4 cs0 = *(LAS f32x4*)(lds + T_CS + (4 * (2 * lw) + g) * 16), cs1 = *(LAS f32x4*)(lds + T_CS + (4 * (2 * lw + 1) + g) * 16);
#pragma unroll
      for (int n = 0; n < 2; ++n) {
        const int qi = 4 * (2 * lw + n) + g;
        const f32x4 cs = n ? cs1 : cs0;
        const int mat = (qi >= 10) + (qi >= 20), rem = qi - 10 * mat, hh = rem >= 5, qd = rem - 5 * hh, par = tt & 1, idx = tt >> 1;
        const int pos = hh ? (qd == 0 ? 4 : qd - 1) : qd;
        float v[4];
#pragma unroll
        for (int i = 0; i < 4; ++i) v[i] = rstd * (hacc[n][i] - mu * cs[i]);
        int off = mat == 2 ? IB_V + (hh * 2 + par) * 4224 + pos * 704 + idx * 8 : IB_K + (hh * 2 + par) * 3840 + (qd >> 1) * 1280 + idx * 16 + (qd & 1) * 8;
        if (qi < 10 || qi >= 30) off = I_DUMMY + lane * 8;
        *(LAS u32x2*)(lds + off) = (u32x2){pkbf(v[0], v[1]), pkbf(v[2], v[3])};
      }
      for (int i = lane; i < 80; i += 64) { *(LAS u32x2*)(lds + IB_Q + lw * 3840 + 2560 + i * 16 + 8) = (u32x2){0u, 0u}; *(LAS u32x2*)(lds + IB_K + lw * 3840 + 2560 + i * 16 + 8) = (u32x2){0u, 0u}; }
      for (int i = lane; i < 88; i += 64) *(LAS u32x2*)(lds + IB_V + lw * 4224 + 5 * 704 + i * 8) = (u32x2){0x3f80u, 0u};
    }
  }
  bar_lgkm();
  TS(5);
  if (!BL) {
#pragma unroll
    for (int i = 0; i < 5; ++i) { const int u = w + 8 * i;
      if (u < 36) {
        const u32x4 v = *(LAS u32x4*)(lds + IMG + u * 1024 + lane * 16);
        unsigned dst;
        if (u < 12) { const int hh = u >= 6; dst = qoff + ((2 * hp + hh) * 64 + 4 * rt) * 1536 + (u - 6 * hh) * 1024; }
        else { const int hh = u >= 24; dst = kvoff + ((2 * hp + hh) * 16 + rt) * KVSTAGE + (u - 12 - 12 * hh) * 1024; }
        __builtin_amdgcn_raw_buffer_store_b128(v, rs, dst + lane * 16, 0, SC1);
      } }
  } else {
    const int hh = w & 1, G = w >> 1, c = lane & 15, g = lane >> 4, q4 = c >> 2, p4 = lane & 3;
    f32x4 bA, bB;
#pragma unroll
    for (int i = 0; i < 4; ++i) { const int r = 4 * g + i;
      bB[i] = (r <= c && r >= c - 7) ? 0.f : -1e30f;
      bA[i] = (r >= c + 9 && !(rt == 0 && G == 0)) ? 0.f : -1e30f; }
    u32x2 o0[2], o1[2];
#pragma unroll
    for (int par = 0; par < 2; ++par) {
      const int im = hh * 2 + par, iB = 8 + 16 * G, iA = iB - 16;
      const int ch = g < 3 ? g : 2;
      bf16x8 qf = lds_b128(lds + IB_Q + im * 3840 + ch * 1280 + (iB + c) * 16);
      if (g == 3) qf = (bf16x8){0, 0, 0, 0, 0, 0, 0, 0};
      const int ia = iA + c < 0 ? 0 : iA + c;
      const bf16x8 kA = lds_b128(lds + IB_K + im * 3840 + ch * 1280 + ia * 16), kB = lds_b128(lds + IB_K + im * 3840 + ch * 1280 + (iB + c) * 16);
      const f32x4 sA = MFMA16(kA, qf, bA), sB = MFMA16(kB, qf, bB);
      float m = __builtin_fmaxf(__builtin_fmaxf(__builtin_fmaxf(sA[0], sA[1]), __builtin_fmaxf(sA[2], sA[3])), __builtin_fmaxf(__builtin_fmaxf(sB[0], sB[1]), __builtin_fmaxf(sB[2], sB[3])));
      m = swap32_max(swap16_max(m));
      const u32x4 pw = {pkbf(ex2(sA[0] - m), ex2(sA[1] - m)), pkbf(ex2(sA[2] - m), ex2(sA[3] - m)), pkbf(ex2(sB[0] - m), ex2(sB[1] - m)), pkbf(ex2(sB[2] - m), ex2(sB[3] - m))};
      const bf16x8 pf = __builtin_bit_cast(bf16x8, pw);
      const int ka = iA + 4 * g + q4 < 0 ? 0 : iA + 4 * g + q4, kb = iB + 4 * g + q4;
      const lchar* vb = lds + IB_V + im * 4224;
      const int qd1 = p4 == 0 ? 4 : 5;
      const f32x4 z = {0.f, 0.f, 0.f, 0.f};
      const f32x4 t0 = MFMA16(cat(lds_tr(vb + p4 * 704 + ka * 8), lds_tr(vb + p4 * 704 + kb * 8)), pf, z);
      const f32x4 t1 = MFMA16(cat(lds_tr(vb + qd1 * 704 + ka * 8), lds_tr(vb + qd1 * 704 + kb * 8)), pf, z);
      auto r1 = __builtin_amdgcn_permlane16_swap(__float_as_uint(t1[0]), __float_as_uint(t1[0]), false, false);
      auto r2 = __builtin_amdgcn_permlane32_swap(r1[1], r1[1], false, false);
      const float inv = 1.0f / __uint_as_float(r2[0]);
      o0[par] = (u32x2){pkbf(t0[0] * inv, t0[1] * inv), pkbf(t0[2] * inv, t0[3] * inv)};
      o1[par] = (u32x2){pkbf(t1[0] * inv, t1[1] * inv), pkbf(t1[2] * inv, t1[3] * inv)};
    }
    const int head = 2 * hp + hh, cA = (5 * head + 1) >> 1;
    const unsigned ob = ooff + (4 * rt + G) * 40960 + (2 * c + (g & 1)) * 16;
    auto rx = __builtin_amdgcn_permlane16_swap(o0[0][0], o0[1][0], false, false), ry = __builtin_amdgcn_permlane16_swap(o0[0][1], o0[1][1], false, false);
    __builtin_amdgcn_raw_buffer_store_b128((u32x4){rx[0], ry[0], rx[1], ry[1]}, rs, ob + (cA + (g >> 1)) * 512, 0, SC1);
    auto sx2 = __builtin_amdgcn_permlane16_swap(o1[0][0], o1[1][0], false, false), sy2 = __builtin_amdgcn_permlane16_swap(o1[0][1], o1[1][1], false, false);
    if (g < 2) __builtin_amdgcn_raw_buffer_store_b64((u32x2){sx2[0], sy2[0]}, rs, ob + (hh ? cA - 1 : cA + 2) * 512 + hh * 8, 0, SC1);
  }
  TS(6);
  bar_vm_lgkm();
  if (tid == 0) flag_set(sy, PH, BL ? rt * 16 + hp : hp * 16 + rt);
  TS(7); TSOUT(PH);
}

constexpr int RS0 = 139264, RS1 = 114688, RS2 = 90112;
DI int rslot(int s) { return RS0 - s * ROUND; }
template <int PH>
DI void phase_attn(lchar* lds, const Params& p, const Sync& sy, unsigned qoff, unsigned kvoff, unsigned ooff, const char* nextw, int x, int head) {
  TSDECL; TS(0);
  const unsigned lds0 = (unsigned)(uintptr_t)lds;
  const int tid = threadIdx.x, lane = tid & 63, w = __builtin_amdgcn_readfirstlane(tid >> 6);
  const int h = lane >> 5, l31 = lane & 31;
  const int qg = w & 3, kh = w >> 2, hp = head >> 1;
  rsrc_t rs = __builtin_amdgcn_make_buffer_rsrc((void*)p.ws, 0, WS_END, 0x00020000);
  const char* kvbase = p.ws + kvoff + (size_t)head * 16 * KVSTAGE;
  const unsigned dvoff = lane * 16;
  auto issue = [&](int r, int slot) { dma3(dvoff, kvbase + w * 3072 + ((x + r) & 7) * ROUND, lds0 + rslot(slot) + w * 3072); };
  if (w == 0) flag_wait(sy, PH - 1, hp * 16 + 2 * x, 2, lane);
  bar();
  TS(1);
  const int tb = 8 * x + 2 * qg;
  const unsigned qb = qoff + (head * 64 + tb) * 1536;
  const bf16x8 qA0 = __builtin_bit_cast(bf16x8, __builtin_amdgcn_raw_buffer_load_b128(rs, qb + h * 512 + l31 * 16, 0, SC1));
  const bf16x8 qB0 = __builtin_bit_cast(bf16x8, __builtin_amdgcn_raw_buffer_load_b128(rs, qb + 1536 + h * 512 + l31 * 16, 0, SC1));
  const u32x2 qa1 = __builtin_amdgcn_raw_buffer_load_b64(rs, qb + 1024 + l31 * 16, 0, SC1);
  const u32x2 qb1 = __builtin_amdgcn_raw_buffer_load_b64(rs, qb + 1536 + 1024 + l31 * 16, 0, SC1);
  const int dg = (lane >> 4) & 1, qq = (lane & 15) >> 2, p4 = lane & 3;
  const int qd = dg ? (p4 == 0 ? 4 : 5) : p4;
  const f32x16 zero = {};
  s16x4 qA1, qB1;
  float o[12];
  for (int attempt = 0;; ++attempt) {
    issue(0, 0);
    asm volatile("s_waitcnt vmcnt(0)\n\ts_barrier" ::: "memory");
    TS(2);
    const lchar* kp0 = lds + RS0 + kh * KVSTAGE + KVK + h * 512 + l31 * 16;
    const lchar* kp1 = lds + RS0 + kh * KVSTAGE + KVK + 1024 + l31 * 16 + h * 8;
    const lchar* vp00 = lds + RS0 + kh * KVSTAGE + KVV + qd * 256 + (((0 + h) ^ qd) * 32) + qq * 8;
    const lchar* vp01 = lds + RS0 + kh * KVSTAGE + KVV + qd * 256 + (((2 + h) ^ qd) * 32) + qq * 8;
    const lchar* vp10 = lds + RS0 + kh * KVSTAGE + KVV + qd * 256 + (((4 + h) ^ qd) * 32) + qq * 8;
    const lchar* vp11 = lds + RS0 + kh * KVSTAGE + KVV + qd * 256 + (((6 + h) ^ qd) * 32) + qq * 8;
    {
      const u32x2 ta = {h ? 0u : qa1[0], h ? 0u : qa1[1]}, tb2 = {h ? 0u : qb1[0], h ? 0u : qb1[1]};
      float ma, mb;
      if (attempt == 0) {
        const bf16x8 a0 = lds_b128(lds + RS0 + KVK + h * 512 + l31 * 16);
        const s16x4 a1 = lds_b64(lds + RS0 + KVK + 1024 + l31 * 16 + h * 8);
        f32x16 sa = MFMA32(a0, qA0, zero); sa = MFMA8(a1, __builtin_bit_cast(s16x4, ta), sa);
        f32x16 sb = MFMA32(a0, qB0, zero); sb = MFMA8(a1, __builtin_bit_cast(s16x4, tb2), sb);
        ma = sa[0]; mb = sb[0];
#pragma unroll
        for (int r = 1; r < 16; ++r) { ma = __builtin_fmaxf(ma, sa[r]); mb = __builtin_fmaxf(mb, sb[r]); }
      } else {
        ma = -INFINITY; mb = -INFINITY;
#pragma unroll 1
        for (int t = 0; t < 64; ++t) {
          const unsigned kb = kvoff + (head * 16 + (t >> 2)) * KVSTAGE + KVK + (t & 3) * 1536;
          const bf16x8 a0 = __builtin_bit_cast(bf16x8, __builtin_amdgcn_raw_buffer_load_b128(rs, kb + h * 512 + l31 * 16, 0, SC1));
          const s16x4 a1 = __builtin_bit_cast(s16x4, __builtin_amdgcn_raw_buffer_load_b64(rs, kb + 1024 + l31 * 16 + h * 8, 0, SC1));
          f32x16 sa = MFMA32(a0, qA0, zero); sa = MFMA8(a1, __builtin_bit_cast(s16x4, ta), sa);
          f32x16 sb = MFMA32(a0, qB0, zero); sb = MFMA8(a1, __builtin_bit_cast(s16x4, tb2), sb);
#pragma unroll
          for (int r = 0; r < 16; ++r) { ma = __builtin_fmaxf(ma, sa[r]); mb = __builtin_fmaxf(mb, sb[r]); }
        }
      }
      ma = swap32_max(ma); mb = swap32_max(mb);
      const unsigned na = pkbf(-ma, 0.f), nb = pkbf(-mb, 0.f);
      const u32x2 ua = {h ? na : qa1[0], h ? 0u : qa1[1]}, ub = {h ? nb : qb1[0], h ? 0u : qb1[1]};
      qA1 = __builtin_bit_cast(s16x4, ua); qB1 = __builtin_bit_cast(s16x4, ub);
    }
    f32x16 oA = {}, oB = {}, sA, sB;
    u32x4 pA0 = {}, pA1 = {}, pB0 = {}, pB1 = {};
    bf16x8 kf0, vf0, vf1; s16x4 kf1;
#define KRD(P0, P1, TL) do { kf0 = lds_b128((P0) + (TL) * 1536); kf1 = lds_b64((P1) + (TL) * 1536); } while (0)
#define VRD(TL) do { vf0 = cat(lds_tr(vp00 + (TL) * 1536), lds_tr(vp01 + (TL) * 1536)); vf1 = cat(lds_tr(vp10 + (TL) * 1536), lds_tr(vp11 + (TL) * 1536)); } while (0)
    KRD(kp0, kp1, 0); VRD(0);
    sA = MFMA32(kf0, qA0, zero); sA = MFMA8(kf1, qA1, sA);
    constexpr int PRIO = 7;
#define SP(Y, g) do { if (Y) { if (((PRIO >> (g)) & 1) != ((PRIO >> (((g) + 3) & 3)) & 1)) __builtin_amdgcn_s_setprio((PRIO >> (g)) & 1); } } while (0)
#define HSTEP(Y, SC, PC0, PC1, SN, QN0, QN1, ON, PN0, PN1, LOADK, LOADV) do { \
    SBAR(); SP(Y, 0); \
    SN = MFMA32(kf0, QN0, zero); SBAR(); \
    SC[0] = ex2(SC[0]); SC[1] = ex2(SC[1]); SC[2] = ex2(SC[2]); SC[3] = ex2(SC[3]); PIN(SC); SBAR(); SP(Y, 1); \
    SN = MFMA8(kf1, QN1, SN); SBAR(); \
    LOADK; \
    SC[4] = ex2(SC[4]); SC[5] = ex2(SC[5]); SC[6] = ex2(SC[6]); SC[7] = ex2(SC[7]); \
    PC0[0] = pkbf(SC[0], SC[1]); PC0[1] = pkbf(SC[2], SC[3]); PIN(SC); PIN(PC0); SBAR(); SP(Y, 2); \
    ON = MFMA32(vf0, __builtin_bit_cast(bf16x8, PN0), ON); SBAR(); \
    SC[8] = ex2(SC[8]); SC[9] = ex2(SC[9]); SC[10] = ex2(SC[10]); SC[11] = ex2(SC[11]); \
    PC0[2] = pkbf(SC[4], SC[5]); PC0[3] = pkbf(SC[6], SC[7]); PIN(SC); PIN(PC0); SBAR(); SP(Y, 3); \
    ON = MFMA32(vf1, __builtin_bit_cast(bf16x8, PN1), ON); SBAR(); \
    LOADV; \
    SC[12] = ex2(SC[12]); SC[13] = ex2(SC[13]); SC[14] = ex2(SC[14]); SC[15] = ex2(SC[15]); \
    PC1[0] = pkbf(SC[8], SC[9]); PC1[1] = pkbf(SC[10], SC[11]); \
    PC1[2] = pkbf(SC[12], SC[13]); PC1[3] = pkbf(SC[14], SC[15]); PIN(PC1); SBAR(); \
  } while (0)
#define NOP_ do {} while (0)
#define XSTEP(Y, LK, LV) HSTEP(Y, sA, pA0, pA1, sB, qB0, qB1, oB, pB0, pB1, LK, LV)
#define YSTEP(Y) HSTEP(Y, sB, pB0, pB1, sA, qA0, qA1, oA, pA0, pA1, NOP_, NOP_)
    int slot = 0;
    auto refill = [&](int r, int sl2) {
      if (r < 6) issue(r + 2, sl2);
      if (r >= 1 && r <= 3) proj_w_issue(lds, nextw, head >> 2, w, lane, 8 * (r - 1), r == 3 ? 22 : 8 * r);
      if (r == 6) proj_w_issue(lds, nextw, head >> 2, w, lane, 22, 25);
    };
#define ROUNDS(Y) \
    for (int r = 0; r < 8; ++r) { \
      const int nslot = slot == 2 ? 0 : slot + 1, n2slot = slot == 0 ? 2 : slot - 1; \
      const int dn = rslot(nslot) - rslot(slot); \
      const lchar* kn0 = kp0 + dn; const lchar* kn1 = kp1 + dn; \
      XSTEP(Y, KRD(kp0, kp1, 1), VRD(0)); YSTEP(Y); \
      if (r == 0) {     \
        if (attempt == 0 && w == 0) flag_wait(sy, PH - 1, hp * 16, 16, lane); \
        if (attempt == 0 && PH == 1 && w == 1) { flag_wait(sy, 0, 0, 64, lane); flag_wait(sy, 0, 64, 64, lane); flag_wait(sy, 0, 128, 64, lane); flag_wait(sy, 0, 192, 64, lane); } \
        bar(); issue(1, 1); \
      } else if (Y) refill(r - 1, nslot); \
      XSTEP(Y, KRD(kp0, kp1, 2), VRD(1)); YSTEP(Y); \
      XSTEP(Y, KRD(kp0, kp1, 3), VRD(2)); YSTEP(Y); \
      asm volatile("s_waitcnt vmcnt(0)\n\ts_barrier" ::: "memory"); \
      if (!Y) refill(r, n2slot); \
      XSTEP(Y, KRD(kn0, kn1, 0), VRD(3)); YSTEP(Y); \
      kp0 = kn0; kp1 = kn1; vp00 += dn; vp01 += dn; vp10 += dn; vp11 += dn; slot = nslot; \
    }
    if (w >= 4) { ROUNDS(true) __builtin_amdgcn_s_setprio(0); } else { ROUNDS(false) }
    oB = MFMA32(vf0, __builtin_bit_cast(bf16x8, pB0), oB);
    oB = MFMA32(vf1, __builtin_bit_cast(bf16x8, pB1), oB);
    TS(3);
    bar_lgkm();
    LAS f32x4* xb = (LAS f32x4*)(lds + RS1) + (qg * 2) * 192 + lane;
    if (kh) { xb[0] = (f32x4){oA[0], oA[1], oA[2], oA[3]}; xb[64] = (f32x4){oA[4], oA[5], oA[6], oA[7]}; xb[128] = (f32x4){oA[8], oA[9], oA[10], oA[11]}; }
    else { xb[192] = (f32x4){oB[0], oB[1], oB[2], oB[3]}; xb[192 + 64] = (f32x4){oB[4], oB[5], oB[6], oB[7]}; xb[192 + 128] = (f32x4){oB[8], oB[9], oB[10], oB[11]}; }
    if (tid == 0) *(LAS unsigned*)(lds + RS0) = 0u;
    bar_lgkm();
    const int sel = kh ? 192 : 0;
    const f32x4 a = xb[sel], b = xb[sel + 64], c = xb[sel + 128];
#pragma unroll
    for (int i = 0; i < 4; ++i) { o[i] = (kh ? oB[i] : oA[i]) + a[i]; o[4 + i] = (kh ? oB[4 + i] : oA[4 + i]) + b[i]; o[8 + i] = (kh ? oB[8 + i] : oA[8 + i]) + c[i]; }
    auto rr = __builtin_amdgcn_permlane32_swap(__float_as_uint(o[8]), __float_as_uint(o[8]), false, false);
    const float l = __uint_as_float(rr[1]);
    const float inv = 1.0f / l;
#pragma unroll
    for (int i = 0; i < 12; ++i) o[i] *= inv;
    if (attempt == 1) break;
    const bool bad = !(l >= 8.6736174e-19f && l <= 1.1529215e18f);
    if (__any(bad) && lane == 0) *(LAS unsigned*)(lds + RS0) = 1u;
    bar_lgkm();
    const unsigned redo = __builtin_amdgcn_readfirstlane(*(LAS unsigned*)(lds + RS0));
    if (!redo) break;
    bar_lgkm();
  }
  const int cA = (5 * head + 1) >> 1, odd = head & 1;
  const unsigned ob = ooff + (tb + kh) * 40960 + l31 * 16;
  __builtin_amdgcn_raw_buffer_store_b64((u32x2){pkbf(o[0], o[1]), pkbf(o[2], o[3])}, rs, ob + cA * 512 + h * 8, 0, SC1);
  __builtin_amdgcn_raw_buffer_store_b64((u32x2){pkbf(o[4], o[5]), pkbf(o[6], o[7])}, rs, ob + (cA + 1) * 512 + h * 8, 0, SC1);
  if (h == 0) __builtin_amdgcn_raw_buffer_store_b64((u32x2){pkbf(o[8], o[9]), pkbf(o[10], o[11])}, rs, ob + (odd ? cA - 1 : cA + 2) * 512 + odd * 8, 0, SC1);
  TS(6);
  bar_vm_lgkm();
  if (tid == 0) flag_set(sy, PH, x * 32 + head);
  TS(7); TSOUT(PH);
}

constexpr int P_RED = 141312;
template <bool FIRST, bool LAST, bool AL, int PH>
DI void phase_proj(lchar* lds, const Params& p, const Sync& sy, const char* W, unsigned ooff, unsigned xin, unsigned xout, unsigned sqout, const char* nextw, int bx, int bj) {
  TSDECL; TS(0);
  const int tid = threadIdx.x, lane = tid & 63, w = __builtin_amdgcn_readfirstlane(tid >> 6);
  const int rt = 4 * bx + (bj & 3), ct = bj >> 2;
  const int wm = w & 3, wk = w >> 2, tok16 = lane & 15, g = lane >> 4;
  rsrc_t rs = __builtin_amdgcn_make_buffer_rsrc((void*)p.ws, 0, WS_END, 0x00020000);
  if (MULTI) proj_w_issue(lds, W, ct, w, lane, 0, 25);
  const int token = 64 * rt + 16 * wm + tok16;
  const unsigned xtok = (token >> 5) * 40960 + (token & 31) * 16;
  u32x2 res[3];
#pragma unroll
  for (int j = 0; j < 3; ++j) { const int t = wk ? 3 + (j & 1) : j; const int quad = 20 * ct + 4 * t + g;
    if (FIRST) res[j] = *(const u32x2*)(p.x0 + (size_t)token * 1280 + quad * 8);
    else res[j] = __builtin_amdgcn_raw_buffer_load_b64(rs, xin + xtok + (quad >> 1) * 512 + (quad & 1) * 8, 0, SC1); }
  if (w == 0) { if (AL) flag_wait(sy, PH - 1, bx * 32, 32, lane); else flag_wait(sy, PH - 1, (rt >> 1) * 16, 16, lane); }
  asm volatile("s_waitcnt vmcnt(0)\n\ts_barrier" ::: "memory");
  TS(1);
  u32x4 a[10];
#pragma unroll
  for (int s = 0; s < 10; ++s) a[s] = __builtin_amdgcn_raw_buffer_load_b128(rs, ooff + xtok + (4 * (10 * wk + s) + g) * 512, 0, SC1);
  TS(2);
  f32x4 acc[5];
#pragma unroll
  for (int t = 0; t < 5; ++t) acc[t] = (f32x4){0.f, 0.f, 0.f, 0.f};
  const int q4 = (lane & 15) >> 2, p4 = lane & 3;
  const lchar* wb = lds + (320 * wk + 16 * (g >> 1) + 4 * (g & 1) + q4) * 160 + p4 * 8;
#pragma unroll
  for (int s = 0; s < 10; ++s) {
    const bf16x8 bf = __builtin_bit_cast(bf16x8, a[s]);
#pragma unroll
    for (int t = 0; t < 5; ++t) acc[t] = MFMA16(cat(lds_tr(wb + s * 5120 + t * 32), lds_tr(wb + s * 5120 + 1280 + t * 32)), bf, acc[t]);
  }
  TS(3);
  LAS f32x4* red = (LAS f32x4*)(lds + P_RED) + wm * 320 + lane;
  if (wk) { red[0] = acc[0]; red[64] = acc[1]; red[128] = acc[2]; } else { red[192] = acc[3]; red[256] = acc[4]; }
  bar_lgkm();
  TS(4);
  float sq = 0.f;
#pragma unroll
  for (int j = 0; j < 3; ++j) {
    if (wk && j == 2) break;
    const int t = wk ? 3 + j : j;
    const f32x4 o = red[t * 64];
    const f32x4 me = wk ? (j == 0 ? acc[3] : acc[4]) : acc[j];
    const float v0 = me[0] + o[0] + bflo(res[j][0]), v1 = me[1] + o[1] + bfhi(res[j][0]), v2 = me[2] + o[2] + bflo(res[j][1]), v3 = me[3] + o[3] + bfhi(res[j][1]);
    const u32x2 pk = {pkbf(v0, v1), pkbf(v2, v3)};
    const int quad = 20 * ct + 4 * t + g;
    if (LAST) *(u32x2*)(p.out + (size_t)token * 1280 + quad * 8) = pk;
    else {
      __builtin_amdgcn_raw_buffer_store_b64(pk, rs, xout + xtok + (quad >> 1) * 512 + (quad & 1) * 8, 0, SC1);
      const float r0 = bflo(pk[0]), r1 = bfhi(pk[0]), r2 = bflo(pk[1]), r3 = bfhi(pk[1]);
      sq += (r0 * r0 + r1 * r1) + (r2 * r2 + r3 * r3);
    }
  }
  if (!LAST) {
    sq = swap32_sum(swap16_sum(sq));
    if (g == 0) __builtin_amdgcn_raw_buffer_store_b32(__float_as_uint(sq), rs, sqout + token * 64 + (2 * ct + wk) * 4, 0, SC1);
  }
  TS(6);
  if (!LAST) {
    bar_vm_lgkm();
    if (tid == 0) flag_set(sy, PH, rt * 8 + ct);
    if (nextw && !MULTI) qkv_prefetch(lds, nextw, bj);
  }
  TS(7); TSOUT(PH);
}

template <int PH> DI void run_phase(lchar* lds, const Params& p, const Sync& sy, int bx, int bj) {
  const char* PWA = p.ws + WS_PW; const char* PWB_ = p.ws + WS_PW + PWB; const char* POA = p.ws + WS_PO; const char* POBp = p.ws + WS_PO + POB_;
  if (PH == 0) phase_qkv<false, true, 0>(lds, p, sy, p.wqA, 0, 0, WS_Q, WS_KV, 0, nullptr, bx, bj);
  if (PH == 1) phase_attn<1>(lds, p, sy, WS_Q, WS_KV, WS_O, POA, bx, bj);
  if (PH == 2) phase_proj<true, false, true, 2>(lds, p, sy, POA, WS_O, 0, WS_X, WS_SQ, PWB_, bx, bj);
  if (PH == 3) phase_qkv<true, false, 3>(lds, p, sy, PWB_, WS_X, WS_SQ, 0, 0, WS_O + XB, MULTI ? nullptr : POBp, bx, bj);
  if (PH == 4) phase_proj<false, false, false, 4>(lds, p, sy, POBp, WS_O + XB, WS_X, WS_X + XB, WS_SQ + SQB, PWA, bx, bj);
  if (PH == 5) phase_qkv<false, false, 5>(lds, p, sy, PWA, WS_X + XB, WS_SQ + SQB, WS_Q + QBYTES, WS_KV + KVBYTES, 0, nullptr, bx, bj);
  if (PH == 6) phase_attn<6>(lds, p, sy, WS_Q + QBYTES, WS_KV + KVBYTES, WS_O + 2 * XB, POA, bx, bj);
  if (PH == 7) phase_proj<false, false, true, 7>(lds, p, sy, POA, WS_O + 2 * XB, WS_X + XB, WS_X + 2 * XB, WS_SQ + 2 * SQB, PWB_, bx, bj);
  if (PH == 8) phase_qkv<true, false, 8>(lds, p, sy, PWB_, WS_X + 2 * XB, WS_SQ + 2 * SQB, 0, 0, WS_O + 3 * XB, MULTI ? nullptr : POBp, bx, bj);
  if (PH == 9) phase_proj<false, true, false, 9>(lds, p, sy, POBp, WS_O + 3 * XB, WS_X + 2 * XB, 0, 0, nullptr, bx, bj);
}

__global__ void __launch_bounds__(512, 2) k_stack(Params p) {
  __shared__ __attribute__((aligned(16))) char smem[163840];
  lchar* lds = (lchar*)smem;
  const int bx = blockIdx.x & 7, bj = blockIdx.x >> 3;
  const u64 nonce = __hip_atomic_load((const GAS u64*)(p.ws + WS_NONCE), __ATOMIC_RELAXED, __HIP_MEMORY_SCOPE_AGENT);
  Sync sy; sy.ws = p.ws;
  sy.tag0 = (((u64)__builtin_amdgcn_readfirstlane((unsigned)(nonce >> 32)) << 32) | __builtin_amdgcn_readfirstlane((unsigned)nonce)) + 1;
  run_phase<0>(lds, p, sy, bx, bj); run_phase<1>(lds, p, sy, bx, bj); run_phase<2>(lds, p, sy, bx, bj); run_phase<3>(lds, p, sy, bx, bj); run_phase<4>(lds, p, sy, bx, bj);
  run_phase<5>(lds, p, sy, bx, bj); run_phase<6>(lds, p, sy, bx, bj); run_phase<7>(lds, p, sy, bx, bj); run_phase<8>(lds, p, sy, bx, bj); run_phase<9>(lds, p, sy, bx, bj);
  if (blockIdx.x == 0 && threadIdx.x == 0) __hip_atomic_store((GAS u64*)(p.ws + WS_NONCE), sy.tag0 + 15, __ATOMIC_RELAXED, __HIP_MEMORY_SCOPE_AGENT);
}

extern "C" void kernel_launch(void* const* d_in, const int* in_sizes, int n_in, void* d_out, int out_size, void* d_ws, size_t ws_size, hipStream_t stream) {
  Params p;
  p.x0 = (const char*)d_in[0]; p.wqA = (const char*)d_in[1]; p.woA = (const char*)d_in[2];
  p.wqB = (const char*)d_in[4]; p.woB = (const char*)d_in[5];
  p.out = (char*)d_out; p.ws = (char*)d_ws;
  k_stack<<<256, 512, 0, stream>>>(p);
}
```

```cpp
#include <hip/hip_runtime.h>
#include <stdint.h>

#define DI __device__ __forceinline__
typedef short bf16x8 __attribute__((ext_vector_type(8)));
typedef short s16x4 __attribute__((ext_vector_type(4)));
typedef short v4i16_t __attribute__((ext_vector_type(4)));
typedef float f32x16 __attribute__((ext_vector_type(16)));
typedef float f32x4 __attribute__((ext_vector_type(4)));
typedef unsigned u32x2 __attribute__((ext_vector_type(2)));
typedef unsigned u32x4 __attribute__((ext_vector_type(4)));
typedef unsigned long long u64;
typedef __bf16 bf16x2_t __attribute__((ext_vector_type(2)));
#define LAS __attribute__((address_space(3)))
#define GAS __attribute__((address_space(1)))
typedef LAS char lchar;
typedef __amdgpu_buffer_rsrc_t rsrc_t;
#define SBAR() __builtin_amdgcn_sched_barrier(0)
#define PIN(x) asm volatile("" : "+v"(x))
#define MFMA32(a, b, c) __builtin_amdgcn_mfma_f32_32x32x16_bf16(a, b, c, 0, 0, 0)
#define MFMA8(a, b, c) __builtin_amdgcn_mfma_f32_32x32x8bf16_1k(a, b, c, 0, 0, 0)
#define MFMA16(a, b, c) __builtin_amdgcn_mfma_f32_16x16x32_bf16(a, b, c, 0, 0, 0)
#define SC1 16

DI unsigned pkbf(float a, float b) { bf16x2_t v = {(__bf16)a, (__bf16)b}; return __builtin_bit_cast(unsigned, v); }
DI float bflo(unsigned u) { return __uint_as_float(u << 16); }
DI float bfhi(unsigned u) { return __uint_as_float(u & 0xffff0000u); }
DI float ex2(float x) { return __builtin_amdgcn_exp2f(x); }
DI float rbf(float x) { return __uint_as_float(pkbf(x, 0.f) << 16); }
DI void ln_scalars(float sx, float sq, float& mu, float& rstd) {
  const float m = sx * (1.0f / 640.0f), var = __builtin_fmaxf(sq * (1.0f / 640.0f) - m * m, 0.f);
  mu = rbf(m);
  rstd = __builtin_amdgcn_rcpf(rbf(__builtin_amdgcn_sqrtf(rbf(rbf(var) + 1e-5f))));
}
DI bf16x8 lds_b128(const lchar* p) { return *(const LAS bf16x8*)p; }
DI s16x4 lds_b64(const lchar* p) { return *(const LAS s16x4*)p; }
DI s16x4 lds_tr(const lchar* p) { return __builtin_bit_cast(s16x4, __builtin_amdgcn_ds_read_tr16_b64_v4i16((LAS v4i16_t*)p)); }
DI bf16x8 cat(s16x4 a, s16x4 b) { return __builtin_shufflevector(a, b, 0, 1, 2, 3, 4, 5, 6, 7); }
DI float swap32_sum(float v) { auto r = __builtin_amdgcn_permlane32_swap(__float_as_uint(v), __float_as_uint(v), false, false); return __uint_as_float(r[0]) + __uint_as_float(r[1]); }
DI float swap32_max(float v) { auto r = __builtin_amdgcn_permlane32_swap(__float_as_uint(v), __float_as_uint(v), false, false); return __builtin_fmaxf(__uint_as_float(r[0]), __uint_as_float(r[1])); }
DI float swap16_sum(float v) { auto r = __builtin_amdgcn_permlane16_swap(__float_as_uint(v), __float_as_uint(v), false, false); return __uint_as_float(r[0]) + __uint_as_float(r[1]); }
DI float swap16_max(float v) { auto r = __builtin_amdgcn_permlane16_swap(__float_as_uint(v), __float_as_uint(v), false, false); return __builtin_fmaxf(__uint_as_float(r[0]), __uint_as_float(r[1])); }

constexpr int S = 2048, D = 640;
constexpr float QSCALE = 0.22360679774997896f * 1.4426950408889634f;
constexpr unsigned XB = S * D * 2;
constexpr int KVSTAGE = 12288;
constexpr int KVK = 0, KVV = 6144, ROUND = 2 * KVSTAGE;
constexpr unsigned QBYTES = 32 * 64 * 1536, KVBYTES = 32 * 16 * KVSTAGE;
constexpr unsigned PWB = 32 * 81920, POB_ = 8 * 102400;
constexpr unsigned WS_FLAG = 0;
constexpr unsigned WS_NONCE = 10 * 256 * 8;
constexpr unsigned WS_X = 32768;
constexpr unsigned WS_O = WS_X + 3 * XB;
constexpr unsigned WS_Q = WS_O + 4 * XB;
constexpr unsigned WS_KV = WS_Q + QBYTES;
constexpr unsigned WS_PW = WS_KV + 2 * KVBYTES;
constexpr unsigned WS_PO = WS_PW + 2 * PWB;
constexpr unsigned WS_SQ = WS_PO + 2 * POB_;
constexpr unsigned SQB = S * 64;
constexpr unsigned WS_END = WS_SQ + 3 * SQB;

struct Params { const char* x0; const char* wqA; const char* woA; const char* wqB; const char* woB; char* out; char* ws; };

DI const char* uni(const char* p) { const uint64_t v = (uint64_t)p; const unsigned lo = __builtin_amdgcn_readfirstlane((unsigned)v), hi = __builtin_amdgcn_readfirstlane((unsigned)(v >> 32)); return (const char*)(((uint64_t)hi << 32) | lo); }
DI unsigned uni(unsigned v) { return __builtin_amdgcn_readfirstlane(v); }
#define GLDS "global_load_lds_dwordx4 "
DI void dma1(unsigned v, const char* sb, unsigned dst) {
  asm volatile("s_mov_b32 m0, %2\n\ts_nop 3\n\t" GLDS "%0, %1 sc1" :: "v"(v), "s"(uni(sb)), "s"(uni(dst)) : "memory"); }
DI void dma2(unsigned v, const char* sb, unsigned dst) {
  asm volatile("s_mov_b32 m0, %2\n\ts_nop 3\n\t" GLDS "%0, %1 sc1\n\t" GLDS "%0, %1 offset:1024 sc1" :: "v"(v), "s"(uni(sb)), "s"(uni(dst)) : "memory"); }
DI void dma3(unsigned v, const char* sb, unsigned dst) {
  asm volatile("s_mov_b32 m0, %2\n\ts_nop 3\n\t" GLDS "%0, %1 sc1\n\t" GLDS "%0, %1 offset:1024 sc1\n\t" GLDS "%0, %1 offset:2048 sc1" :: "v"(v), "s"(uni(sb)), "s"(uni(dst)) : "memory"); }
DI void dma4(unsigned v, const char* sb, unsigned dst) {
  asm volatile("s_mov_b32 m0, %2\n\ts_nop 3\n\t" GLDS "%0, %1 sc1\n\t" GLDS "%0, %1 offset:1024 sc1\n\t" GLDS "%0, %1 offset:2048 sc1\n\t" GLDS "%0, %1 offset:3072 sc1"
               :: "v"(v), "s"(uni(sb)), "s"(uni(dst)) : "memory"); }
DI void dma4v(unsigned v0, unsigned v1, unsigned v2, unsigned v3, const char* sb, unsigned dst) {
  asm volatile("s_mov_b32 m0, %5\n\ts_nop 3\n\t" GLDS "%0, %4\n\t" GLDS "%1, %4 offset:1024\n\t" GLDS "%2, %4 offset:2048\n\t" GLDS "%3, %4 offset:3072"
               :: "v"(v0), "v"(v1), "v"(v2), "v"(v3), "s"(uni(sb)), "s"(uni(dst)) : "memory"); }
DI void dma4vm(unsigned v0, unsigned v1, unsigned v2, unsigned v3, const char* sb, unsigned dst, u64 m0_, u64 m1, u64 m2, u64 m3) {
  asm volatile("s_mov_b32 m0, %5\n\ts_mov_b64 exec, %6\n\ts_nop 2\n\t" GLDS "%0, %4\n\ts_mov_b64 exec, %7\n\t" GLDS "%1, %4 offset:1024\n\ts_mov_b64 exec, %8\n\t" GLDS "%2, %4 offset:2048\n\t"
               "s_mov_b64 exec, %9\n\t" GLDS "%3, %4 offset:3072\n\ts_mov_b64 exec, -1"
               :: "v"(v0), "v"(v1), "v"(v2), "v"(v3), "s"(uni(sb)), "s"(uni(dst)), "s"(m0_), "s"(m1), "s"(m2), "s"(m3) : "memory"); }
DI void l2_touch(const char* base, unsigned dst, int u, int lane) {
  asm volatile("s_mov_b32 m0, %2\n\ts_nop 3\n\tglobal_load_lds_dword %0, %1 sc1" :: "v"(u * 8192 + lane * 128), "s"(uni(base)), "s"(uni(dst)) : "memory"); }
template <int N> DI void wait_vm() { asm volatile("s_waitcnt vmcnt(%0)" :: "i"(N) : "memory"); }
DI void bar() { asm volatile("s_barrier" ::: "memory"); }
DI void bar_lgkm() { asm volatile("s_waitcnt lgkmcnt(0)\n\ts_barrier" ::: "memory"); }
DI void bar_vm_lgkm() { asm volatile("s_waitcnt vmcnt(0) lgkmcnt(0)\n\ts_barrier" ::: "memory"); }

constexpr int RELAY = 161792;
struct Sync { const char* ws; u64 nonce; const char* code; int clen; };
DI u64 tag_of(const Sync& sy, int ph) { return ((((u64)__builtin_amdgcn_readfirstlane((unsigned)(sy.nonce >> 32))) << 32) | __builtin_amdgcn_readfirstlane((unsigned)sy.nonce)) + 1 + ph; }
DI void flag_set(const Sync& sy, int ph, int idx) {
  __hip_atomic_store((GAS u64*)(sy.ws + WS_FLAG + (ph * 256 + idx) * 8), tag_of(sy, ph), __ATOMIC_RELAXED, __HIP_MEMORY_SCOPE_AGENT);
}
DI void code_touch(const Sync& sy, unsigned dst, int bj, int lane) {
  const int o = (bj * 32 + (lane & 31)) * 128, last = sy.clen - 4;
  asm volatile("s_mov_b32 m0, %2\n\ts_nop 3\n\tglobal_load_lds_dword %0, %1" :: "v"(o < last ? o : last), "s"(uni(sy.code)), "s"(uni(dst)) : "memory"); }
DI void flag_wait(const Sync& sy, int ph, int idx, int n, int lane) {
  const GAS u64* f = (const GAS u64*)(sy.ws + WS_FLAG + (ph * 256 + idx + (lane < n ? lane : n - 1)) * 8);
  const u64 want = tag_of(sy, ph);
  for (int spin = 0; spin < 40000; ++spin) {
    const u64 v = __hip_atomic_load(f, __ATOMIC_RELAXED, __HIP_MEMORY_SCOPE_AGENT);
    if (__all(v == want)) break;
    __builtin_amdgcn_s_sleep(2);
  }
  asm volatile("" ::: "memory");
}

DI void flag_wait_relay(const Sync& sy, int ph, int idx, int n, int lane, bool lead, lchar* word) {
  if (lead) { flag_wait(sy, ph, idx, n, lane); if (lane == 0) *(volatile LAS unsigned*)word = (unsigned)ph + 1u; }
  else { for (int spin = 0; spin < 4000000; ++spin) { if (__builtin_amdgcn_readfirstlane(*(volatile LAS unsigned*)word) == (unsigned)ph + 1u) break; __builtin_amdgcn_s_sleep(1); } }
  asm volatile("" ::: "memory");
}

constexpr int QSLOT = 34816, QA = 0, QW = 16384;
constexpr int T_SQ = 162048, T_SX = T_SQ + 640, T_CS = T_SX + 640;
constexpr int IMG = 104448;
constexpr int IA_Q = IMG, IA_KV = IMG + 12288, I_DUMMY = IMG + 49152;
constexpr int TOUCH0 = 150016;

DI void proj_w_issue(lchar* lds, const char* pw, int ct, int w, int lane, int g0, int g1) {
  const unsigned lds0 = (unsigned)(uintptr_t)lds;
  for (int g = g0 + w; g < g1; g += 8) dma4(lane * 16, pw + (size_t)ct * 102400 + g * 4096, lds0 + g * 4096);
}
DI void qkv_prefetch(lchar* lds, const char* PW, int head, int part) {
  const int lane = threadIdx.x & 63, w = __builtin_amdgcn_readfirstlane(threadIdx.x >> 6);
  const unsigned lds0 = (unsigned)(uintptr_t)lds;
  if (part == 0) {
#pragma unroll
    for (int i = 0; i < 3; ++i) { const int k0 = w + 8 * i, k = k0 < 19 ? k0 : 18; dma2(lane * 16, PW + (size_t)head * 81920 + k * 2048, lds0 + 102400 + k * 2048); } }
  else if (w >= 4) { for (int k = w - 4; k < 21; k += 4) dma2(lane * 16, PW + (size_t)head * 81920 + (19 + k) * 2048, lds0 + k * 2048); }
}

DI void phase_first(lchar* lds, const Params& p, const Sync& sy, int bx, int bj) {
  const int tid = threadIdx.x, lane = tid & 63, w = __builtin_amdgcn_readfirstlane(tid >> 6);
  const unsigned lds0 = (unsigned)(uintptr_t)lds;
  const int rt = 2 * bx + (bj & 1), hp = bj >> 1;
  const int h = lane >> 5, l31 = lane & 31;
  const char* W = p.wqA;
  rsrc_t rs = __builtin_amdgcn_make_buffer_rsrc((void*)p.ws, 0, WS_END, 0x00020000);
  f32x16 acc[2][2];
  int eo[2][4];
  if (w >= 4) {
    const int lw = w - 4;
    unsigned wv[4], av[4]; u64 wmk[4];
#pragma unroll
    for (int i = 0; i < 4; ++i) { const int q = lane >> 4, c = (lane & 15) ^ ((q << 2) | i), cc = c == 15 ? 14 : c;
      wv[i] = (q + 4 * i) * 3840 + (cc / 5) * 1280 + 80 * hp + (cc % 5) * 16 - 1024 * i; wmk[i] = __ballot(c != 15);
      av[i] = ((lane >> 3) + 8 * i) * 1280 + (((lane & 7) ^ ((4 * i + (lane >> 4)) & 7)) * 16) - 1024 * i; }
    auto issue = [&](int t) {
      dma4vm(wv[0], wv[1], wv[2], wv[3], W + (size_t)(64 * t + 16 * lw) * 3840, lds0 + (t & 3) * QSLOT + QW + lw * 4096, wmk[0], wmk[1], wmk[2], wmk[3]);
      dma4v(av[0], av[1], av[2], av[3], p.x0 + (size_t)(128 * rt + 32 * lw) * 1280 + t * 128, lds0 + (t & 3) * QSLOT + QA + lw * 4096);
    };
#pragma unroll
    for (int t = 0; t < 4; ++t) issue(t);
    const int g = lane >> 4, q4 = (lane & 15) >> 2, p4 = lane & 3;
    const lchar* hw[2][2];
#pragma unroll
    for (int n = 0; n < 2; ++n)
#pragma unroll
      for (int tt = 0; tt < 2; ++tt) { const int row = 8 * g + 4 * tt + q4, ch = 2 * (2 * lw + n) + (p4 >> 1);
        hw[n][tt] = lds + QW + row * 256 + (ch ^ (((row & 3) << 2) | ((row >> 2) & 3))) * 16 + 8 * (p4 & 1); }
    const lchar* sx_[2];
#pragma unroll
    for (int s = 0; s < 2; ++s) sx_[s] = lds + QA + (32 * lw + (lane & 15)) * 128 + (((4 * s + g) ^ (((lane & 15) >> 1) & 7)) * 16);
    f32x4 xx[2] = {{0.f, 0.f, 0.f, 0.f}, {0.f, 0.f, 0.f, 0.f}}, hacc[2] = {{0.f, 0.f, 0.f, 0.f}, {0.f, 0.f, 0.f, 0.f}};
    const bf16x8 ones8 = {0x3f80, 0x3f80, 0x3f80, 0x3f80, 0x3f80, 0x3f80, 0x3f80, 0x3f80};
#define HELP(T) do { const int so = ((T) & 3) * QSLOT; bf16x8 hwf[2][2]; \
      _Pragma("unroll") for (int s = 0; s < 2; ++s) _Pragma("unroll") for (int n = 0; n < 2; ++n) hwf[s][n] = cat(lds_tr(hw[n][0] + so + s * 8192), lds_tr(hw[n][1] + so + s * 8192)); \
      _Pragma("unroll") for (int s = 0; s < 2; ++s) _Pragma("unroll") for (int n = 0; n < 2; ++n) hacc[n] = MFMA16(hwf[s][n], ones8, hacc[n]); \
      _Pragma("unroll") for (int s = 0; s < 2; ++s) _Pragma("unroll") for (int m = 0; m < 2; ++m) { const bf16x8 f_ = lds_b128(sx_[s] + so + m * 2048); xx[m] = MFMA16(f_, f_, xx[m]); } } while (0)
#define LSTAGE(T, N) do { wait_vm<N>(); bar(); HELP((T) + 1); if ((T) <= 5) { bar_lgkm(); issue((T) + 4); } } while (0)
    wait_vm<24>(); bar_lgkm(); HELP(0);
    LSTAGE(0, 16); LSTAGE(1, 16); LSTAGE(2, 16); LSTAGE(3, 16); LSTAGE(4, 16); LSTAGE(5, 16); LSTAGE(6, 16); LSTAGE(7, 8); LSTAGE(8, 0);
#undef HELP
#undef LSTAGE
    const int c = lane & 15;
#pragma unroll
    for (int m = 0; m < 2; ++m) { const float d = (c & 2) ? ((c & 1) ? xx[m][3] : xx[m][2]) : ((c & 1) ? xx[m][1] : xx[m][0]);
      if (g == (c >> 2)) *(LAS float*)(lds + T_SQ + (16 + 32 * lw + 16 * m + c) * 4) = d; }
    if (c == 0) { *(LAS f32x4*)(lds + T_CS + (4 * (2 * lw) + g) * 16) = hacc[0]; *(LAS f32x4*)(lds + T_CS + (4 * (2 * lw + 1) + g) * 16) = hacc[1]; }
  } else {
    const int wm = w >> 1, wn = w & 1;
    {
      const int L = blockIdx.x * 155 + w * 39 + (lane < 39 ? lane : 38), Lc = L < 39680 ? L : 39679;
      const char* a_ = Lc < 19200 ? W + (size_t)Lc * 128 : p.x0 + (size_t)(Lc - 19200) * 128;
      asm volatile("s_mov_b32 m0, %1\n\ts_nop 3\n\tglobal_load_lds_dword %0, off" :: "v"(a_), "s"(uni(lds0 + TOUCH0 + 256 + w * 256)) : "memory"); }
    { const int pc = 15 ^ (((lane & 3) << 2) | ((lane >> 2) & 3));
      *(LAS u32x4*)(lds + w * QSLOT + QW + lane * 256 + pc * 16) = (u32x4){0x3f803f80u, 0x3f803f80u, 0x3f803f80u, 0x3f803f80u}; }
    const int gw = blockIdx.x * 4 + w;
    u32x4 pk[7];
#pragma unroll
    for (int k = 0; k < 5; ++k) {
      const int u = gw + 1024 * k, m = u >= 2560, r = u - 2560 * m, hd = r / 80, row = 8 * (r % 80) + (lane >> 3), lc = (lane & 7) ^ (((lane >> 4) & 1) << 2);
      const char* rowp = (m ? p.wqB : p.wqA) + (size_t)row * 3840 + hd * 40;
      const int qa = 2 * lc, qb = lc == 7 ? 14 : 2 * lc + 1, ma = qa / 5, mb = qb / 5, pa = qa - 5 * ma, pb = qb - 5 * mb;
      const int da = (hd & 1) ? (pa == 4 ? 0 : pa + 1) : pa, db = (hd & 1) ? (pb == 4 ? 0 : pb + 1) : pb;
      const u32x2 lo = *(const u32x2*)(rowp + ma * 1280 + da * 8), hi = *(const u32x2*)(rowp + mb * 1280 + db * 8);
      pk[k] = (u32x4){lo[0], lo[1], hi[0], hi[1]};
    }
#pragma unroll
    for (int k = 5; k < 7; ++k) {
      const int u = gw + 1024 * k, v = (u < 6720 ? u : 5120) - 5120, m = v >= 800, r = v - 800 * m, ct = r / 100, idx = 64 * (r % 100) + lane, pr = idx / 10, ch = idx - 10 * pr;
      const int row = (pr & ~12) | ((pr & 4) << 1) | ((pr & 8) >> 1);
      pk[k] = *(const u32x4*)((m ? p.woB : p.woA) + (size_t)row * 1280 + ct * 160 + ch * 16);
    }
    const lchar* xa[2][4];
#pragma unroll
    for (int a = 0; a < 2; ++a) { const int mt = a ? 1 - wn : wn;
#pragma unroll
      for (int ks = 0; ks < 4; ++ks) xa[a][ks] = lds + QA + (64 * wm + 32 * mt + l31) * 128 + (((2 * ks + h) ^ ((l31 >> 1) & 7)) * 16); }
    const int blk = (lane >> 4) & 1, q4 = (lane & 15) >> 2, p4 = lane & 3;
    const lchar* wa[2][2];
#pragma unroll
    for (int b = 0; b < 2; ++b) { const int nt = b ? 1 - wm : wm;
#pragma unroll
      for (int tt = 0; tt < 2; ++tt) { const int row = 8 * h + 4 * tt + q4, ch = 4 * (2 * wn + nt) + 2 * blk + (p4 >> 1);
        wa[b][tt] = lds + QW + row * 256 + (ch ^ (((row & 3) << 2) | ((row >> 2) & 3))) * 16 + 8 * (p4 & 1); } }
#pragma unroll
    for (int a = 0; a < 2; ++a)
#pragma unroll
      for (int b = 0; b < 2; ++b) acc[a][b] = (f32x16){};
    bf16x8 xf[4][2], wf[4][2];
#define XRD(BUF, P, KS, a) xf[BUF][a] = lds_b128(P[a][KS])
#define WRD(BUF, P, KS, b) wf[BUF][b] = cat(lds_tr(P[b][0] + (KS) * 4096), lds_tr(P[b][1] + (KS) * 4096))
#define KSTEP(C, N, PX, PW, KS) do { \
      acc[0][0] = MFMA32(wf[C][0], xf[C][0], acc[0][0]); SBAR(); \
      XRD(N, PX, KS, 0); WRD(N, PW, KS, 0); SBAR(); \
      acc[0][1] = MFMA32(wf[C][1], xf[C][0], acc[0][1]); SBAR(); \
      WRD(N, PW, KS, 1); SBAR(); \
      acc[1][0] = MFMA32(wf[C][0], xf[C][1], acc[1][0]); SBAR(); \
      XRD(N, PX, KS, 1); SBAR(); \
      acc[1][1] = MFMA32(wf[C][1], xf[C][1], acc[1][1]); SBAR(); } while (0)
#pragma unroll
    for (int b = 0; b < 2; ++b)
#pragma unroll
      for (int g = 0; g < 4; ++g) {
        const int qi = 8 * (2 * wn + (b ? 1 - wm : wm)) + 2 * g + h;
        const int mat = (qi >= 10) + (qi >= 20), rem = qi - 10 * mat, hh = rem >= 5, qd = rem - 5 * hh;
        const int pos = hh ? (qd == 0 ? 4 : qd - 1) : qd;
        const int qk = (mat ? IA_KV + hh * 12288 : IA_Q + hh * 6144) + (pos >> 1) * 512 + l31 * 16 + (pos & 1) * 8;
        const int vv = IA_KV + hh * 12288 + KVV + pos * 256 + (((l31 >> 2) ^ pos) * 32) + (l31 & 3) * 8;
        eo[b][g] = qi >= 30 ? (I_DUMMY + lane * 8) | 4 : (mat == 2 ? vv : qk) | (mat == 0 ? 2 : 0);
        PIN(eo[b][g]);
      }
    bar_lgkm();
    XRD(0, xa, 0, 0); XRD(0, xa, 0, 1); WRD(0, wa, 0, 0); WRD(0, wa, 0, 1);
    XRD(1, xa, 1, 0); XRD(1, xa, 1, 1); WRD(1, wa, 1, 0); WRD(1, wa, 1, 1); SBAR();
#pragma unroll 1
    for (int T = 0; T < 10; ++T) {
      const int dn = (T & 3) == 3 ? -3 * QSLOT : QSLOT;
      const lchar* xn[2][4]; const lchar* wn_[2][2];
#pragma unroll
      for (int a = 0; a < 2; ++a) {
#pragma unroll
        for (int ks = 0; ks < 4; ++ks) xn[a][ks] = xa[a][ks] + dn;
        wn_[a][0] = wa[a][0] + dn; wn_[a][1] = wa[a][1] + dn; }
      KSTEP(0, 2, xa, wa, 2); KSTEP(1, 3, xa, wa, 3);
      if (T < 9) bar();
      KSTEP(2, 0, xn, wn_, 0); KSTEP(3, 1, xn, wn_, 1);
      if (T <= 5) bar();
#pragma unroll
      for (int a = 0; a < 2; ++a) {
#pragma unroll
        for (int ks = 0; ks < 4; ++ks) xa[a][ks] = xn[a][ks];
        wa[a][0] = wn_[a][0]; wa[a][1] = wn_[a][1]; }
    }
#undef XRD
#undef WRD
#undef KSTEP
    const bool onesq = ((lane & 7) ^ (((lane >> 4) & 1) << 2)) == 7;
#pragma unroll
    for (int k = 0; k < 5; ++k) { pk[k][2] = onesq ? 0x3f803f80u : pk[k][2]; pk[k][3] = onesq ? 0x3f803f80u : pk[k][3]; }
    asm volatile("" : "+v"(pk[0]), "+v"(pk[1]), "+v"(pk[2]), "+v"(pk[3]), "+v"(pk[4]), "+v"(pk[5]), "+v"(pk[6]));
#pragma unroll
    for (int k = 0; k < 7; ++k) { const int u = gw + 1024 * k;
      const unsigned dst = k < 5 ? WS_PW + u * 1024 : (u < 6720 ? WS_PO + (u - 5120) * 1024 : WS_END);
      __builtin_amdgcn_raw_buffer_store_b128(pk[k], rs, dst + lane * 16, 0, SC1); }
    if (lane < 32 && wn == 1) {
      *(LAS float*)(lds + T_SX + (16 + 64 * wm + 32 + lane) * 4) = wm ? acc[0][0][12] : acc[0][1][12];
      *(LAS float*)(lds + T_SX + (16 + 64 * wm + lane) * 4) = wm ? acc[1][0][12] : acc[1][1][12];
    }
  }
  bar_lgkm();
  if (w < 4) {
    const int wm = w >> 1, wn = w & 1;
    f32x4 cs[2][4]; float mu[2], rstd[2];
#pragma unroll
    for (int b = 0; b < 2; ++b)
#pragma unroll
      for (int g = 0; g < 4; ++g) cs[b][g] = *(LAS f32x4*)(lds + T_CS + (8 * (2 * wn + (b ? 1 - wm : wm)) + 2 * g + h) * 16);
#pragma unroll
    for (int a = 0; a < 2; ++a) { const int tt = 16 + 32 * (2 * wm + (a ? 1 - wn : wn)) + l31;
      ln_scalars(*(LAS float*)(lds + T_SX + tt * 4), *(LAS float*)(lds + T_SQ + tt * 4), mu[a], rstd[a]); }
    SBAR();
#pragma unroll
    for (int b = 0; b < 2; ++b)
#pragma unroll
      for (int g = 0; g < 4; ++g) {
        const int e = eo[b][g], base = e & ~7;
        const float sc = (e & 2) ? QSCALE : 1.0f;
        const int tstr = (e & 4) ? 0 : 1536;
#pragma unroll
        for (int a = 0; a < 2; ++a) {
          const int tl = 2 * wm + (a ? 1 - wn : wn);
          const float r = rstd[a] * sc, rm = -r * mu[a];
          const u32x2 o2 = {pkbf(__builtin_fmaf(r, acc[a][b][4 * g], rm * cs[b][g][0]), __builtin_fmaf(r, acc[a][b][4 * g + 1], rm * cs[b][g][1])),
                            pkbf(__builtin_fmaf(r, acc[a][b][4 * g + 2], rm * cs[b][g][2]), __builtin_fmaf(r, acc[a][b][4 * g + 3], rm * cs[b][g][3]))};
          *(LAS u32x2*)(lds + base + tl * tstr) = o2;
        }
      }
  } else {
    const int lw = w - 4, hh = lw >> 1;
#pragma unroll
    for (int i = 0; i < 2; ++i) { const int tl = 2 * (lw & 1) + i;
      if (h == 0) *(LAS u32x2*)(lds + IA_KV + hh * 12288 + tl * 1536 + 1024 + l31 * 16 + 8) = (u32x2){0x3f80u, 0u};
      else *(LAS u32x2*)(lds + IA_KV + hh * 12288 + KVV + tl * 1536 + 5 * 256 + l31 * 8) = (u32x2){0x3f80u, 0u}; }
  }
  bar_lgkm();
  u32x4 cv[5];
#pragma unroll
  for (int i = 0; i < 5; ++i) { const int u = w + 8 * i; cv[i] = *(LAS u32x4*)(lds + IMG + (u < 36 ? u : 0) * 1024 + lane * 16); }
#pragma unroll
  for (int i = 0; i < 5; ++i) { const int u = w + 8 * i;
    if (i < 4 || w < 4) {
      unsigned dst;
      if (u < 12) { const int hh = u >= 6; dst = WS_Q + ((2 * hp + hh) * 64 + 4 * rt) * 1536 + (u - 6 * hh) * 1024; }
      else { const int hh = u >= 24; dst = WS_KV + ((2 * hp + hh) * 16 + rt) * KVSTAGE + (u - 12 - 12 * hh) * 1024; }
      __builtin_amdgcn_raw_buffer_store_b128(cv[i], rs, dst + lane * 16, 0, SC1);
    } }
  bar_vm_lgkm();
  if (tid == 0) flag_set(sy, 0, hp * 16 + rt);
}

constexpr int RS0 = 139264, RS1 = 114688, RS2 = 90112;
constexpr int W1 = 102400, WKS1 = 19;
constexpr int U_HX = 43008;
constexpr int U_CS = 81920, U_SXH = U_CS + 256, U_DUMMY = U_CS + 512, U_FLG = U_CS + 1024;
DI int wks_off(int ks) { return ks < WKS1 ? W1 + ks * 2048 : (ks - WKS1) * 2048; }
constexpr int UA_Q = RS1;
constexpr int UB_Q = 90112, UB_K = UB_Q + 13824, UB_V = UB_K + 13824;
template <bool BL, int PH>
DI void phase_qkv2(lchar* lds, const Params& p, const Sync& sy, unsigned xoff, unsigned sqoff, unsigned kvoff, unsigned ooff, const char* nextw, const char* touchw, int x, int head) {
  const int tid = threadIdx.x, lane = tid & 63, w = __builtin_amdgcn_readfirstlane(tid >> 6);
  const int h = lane >> 5, l31 = lane & 31;
  rsrc_t rs = __builtin_amdgcn_make_buffer_rsrc((void*)p.ws, 0, WS_END, 0x00020000);
  f32x16 acc[2][2];
  f32x4 hacc = {0.f, 0.f, 0.f, 0.f};
  float sqv[2] = {0.f, 0.f};
  if (w >= 4) {
    const int lw = w - 4, c = lane & 15, g = lane >> 4, q4 = c >> 2, p4 = lane & 3;
    const lchar* hw0 = lds + (8 * (g & 1) + q4) * 128 + (((lw >> 1) ^ ((q4 >> 1) & 1)) * 64) + 32 * (lw & 1) + 8 * p4;
    const int gk = g >> 1;
    const bf16x8 ones8 = {0x3f80, 0x3f80, 0x3f80, 0x3f80, 0x3f80, 0x3f80, 0x3f80, 0x3f80};
    f32x4 hf = {0.f, 0.f, 0.f, 0.f};
    if (BL) {
      const int htok = x == 0 ? 16 : 256 * x - 16;
      flag_wait(sy, PH - 1, x > 0 ? 32 * x - 8 : 0, 8, lane);

      const char* hb = p.ws + xoff + (size_t)(htok >> 5) * 40960 + lw * 5 * 2048;
      const unsigned hv = g * 512 + 256 + c * 16, hd = (unsigned)(uintptr_t)lds + U_HX + lw * 5 * 1024;
#pragma unroll
      for (int i = 0; i < 5; ++i) dma1(hv, hb + i * 2048, hd + i * 1024);
      asm volatile("s_waitcnt vmcnt(5)\n\ts_barrier" ::: "memory");
#pragma unroll
      for (int i = 0; i < 4; ++i) hf[i] = __uint_as_float(__builtin_amdgcn_raw_buffer_load_b32(rs, sqoff + (htok >> 5) * 2048 + (4 * g + i) * 128 + (16 + c) * 4, 0, SC1));
      asm volatile("s_waitcnt vmcnt(0)" : "+v"(hf) :: "memory");
      if (lane == 0) *(volatile LAS unsigned*)(lds + U_FLG + lw * 4) = 1u;
      for (int spin = 0; spin < 1000000; ++spin) { const unsigned f = *(volatile LAS unsigned*)(lds + U_FLG + (lane & 3) * 4); if (__all(f == 1u)) break; __builtin_amdgcn_s_sleep(1); }
      asm volatile("" ::: "memory");
      if (touchw) { l2_touch(touchw + (size_t)head * 81920, (unsigned)(uintptr_t)lds + 65536, lw, lane); if (lw == 0) l2_touch(touchw + (size_t)head * 81920, (unsigned)(uintptr_t)lds + 65536, 4, lane); }
    } else asm volatile("s_waitcnt vmcnt(0)\n\ts_barrier" ::: "memory");
    __builtin_amdgcn_s_setprio(2);
    f32x4 hacc2 = {0.f, 0.f, 0.f, 0.f};
#pragma unroll
    for (int b5 = 0; b5 < 4; ++b5) {
      bf16x8 wfr[5], hxf[5];
#pragma unroll
      for (int i = 0; i < 5; ++i) { const int s = 5 * b5 + i;
        if (BL) hxf[i] = lds_b128(lds + U_HX + (4 * s + g) * 256 + c * 16);
        const int o0_ = wks_off(2 * s), o1_ = wks_off(2 * s + 1);
        const lchar* a_ = hw0 + (gk ? o1_ : o0_);
        wfr[i] = cat(lds_tr(a_), lds_tr(a_ + 512)); }
      SBAR();
#pragma unroll
      for (int i = 0; i < 5; ++i) { const int s = 5 * b5 + i;
        const bf16x8 xf = BL ? (c == 0 ? ones8 : hxf[i]) : ones8;
        if (i & 1) hacc2 = MFMA16(wfr[i], xf, hacc2); else hacc = MFMA16(wfr[i], xf, hacc); }
      SBAR();
    }
    __builtin_amdgcn_s_setprio(0);
#pragma unroll
    for (int i = 0; i < 4; ++i) hacc[i] += hacc2[i];
    if (BL) {
      sqv[0] = swap32_sum(swap16_sum((hf[0] + hf[1]) + (hf[2] + hf[3])));
      if (lw == 3 && g == 3) *(LAS float*)(lds + U_SXH + c * 4) = hacc[0];
    }
    if (c == 0) *(LAS f32x4*)(lds + U_CS + (4 * lw + g) * 16) = hacc;
    if (BL) {
      const int o = ((head & 3) * 200 + lw * 50 + (lane < 50 ? lane : 49)) * 128;
      asm volatile("s_mov_b32 m0, %2\n\ts_nop 3\n\tglobal_load_lds_dword %0, %1 sc1" :: "v"(o), "s"(uni(nextw + (size_t)(head >> 2) * 102400)), "s"(uni((unsigned)(uintptr_t)lds + 66560)) : "memory"); }

  } else {
    flag_wait_relay(sy, PH - 1, 32 * x, 32, lane, w == 0, lds + RELAY);

    const unsigned xv0 = h * 512 + l31 * 16, xv1 = xv0 + 40960, xs = xoff + (8 * x + 2 * w) * 40960;
    constexpr int DEP = 8;
    u32x4 xr[DEP][2];
#pragma unroll
    for (int i = 0; i < DEP; ++i) { xr[i][0] = __builtin_amdgcn_raw_buffer_load_b128(rs, xv0, xs + i * 1024, SC1); xr[i][1] = __builtin_amdgcn_raw_buffer_load_b128(rs, xv1, xs + i * 1024, SC1); }
    SBAR();
    unsigned sqp[2][8];
#pragma unroll
    for (int a = 0; a < 2; ++a)
#pragma unroll
      for (int i = 0; i < 8; ++i) sqp[a][i] = __builtin_amdgcn_raw_buffer_load_b32(rs, sqoff + (8 * x + 2 * w + a) * 2048 + (8 * h + i) * 128 + l31 * 4, 0, SC1);
    SBAR();
    const int blk = (lane >> 4) & 1, q4 = (lane & 15) >> 2, p4 = lane & 3;
    const lchar* wa[2]; const lchar* wb_[2];
#pragma unroll
    for (int nt = 0; nt < 2; ++nt) { wa[nt] = lds + (8 * h + q4) * 128 + ((nt ^ ((q4 >> 1) & 1)) * 64) + 32 * blk + 8 * p4; wb_[nt] = wa[nt] + W1; PIN(wb_[nt]); }
#pragma unroll
    for (int a = 0; a < 2; ++a)
#pragma unroll
      for (int b = 0; b < 2; ++b) acc[a][b] = (f32x16){};
    bf16x8 wf[3][2];
#define WPTR(KS, nt) ((KS) < WKS1 ? wb_[nt] + (KS) * 2048 : wa[nt] + ((KS) - WKS1) * 2048)
#define WRD2(BUF, KS, nt) wf[BUF][nt] = cat(lds_tr(WPTR(KS, nt)), lds_tr(WPTR(KS, nt) + 512))
    WRD2(0, 0, 0); WRD2(0, 0, 1); WRD2(1, 1, 0); WRD2(1, 1, 1); SBAR();

#pragma unroll
    for (int ks = 0; ks < 40; ++ks) {
      if (ks == 12) bar();
      const bf16x8 x0 = __builtin_bit_cast(bf16x8, xr[ks % DEP][0]), x1 = __builtin_bit_cast(bf16x8, xr[ks % DEP][1]);
      acc[0][0] = MFMA32(wf[ks % 3][0], x0, acc[0][0]); SBAR();
      if (ks + 2 < 40) { WRD2((ks + 2) % 3, ks + 2, 0); } SBAR();
      acc[0][1] = MFMA32(wf[ks % 3][1], x0, acc[0][1]); SBAR();
      if (ks + DEP < 40) xr[ks % DEP][0] = __builtin_amdgcn_raw_buffer_load_b128(rs, xv0, xs + (ks + DEP) * 1024, SC1);
      SBAR();
      acc[1][0] = MFMA32(wf[ks % 3][0], x1, acc[1][0]); SBAR();
      if (ks + 2 < 40) { WRD2((ks + 2) % 3, ks + 2, 1); } SBAR();
      acc[1][1] = MFMA32(wf[ks % 3][1], x1, acc[1][1]); SBAR();
      if (ks + DEP < 40) xr[ks % DEP][1] = __builtin_amdgcn_raw_buffer_load_b128(rs, xv1, xs + (ks + DEP) * 1024, SC1);
      SBAR();
    }

#pragma unroll
    for (int a = 0; a < 2; ++a) { float t = 0.f;
#pragma unroll
      for (int i = 0; i < 8; ++i) t += __uint_as_float(sqp[a][i]);
      sqv[a] = swap32_sum(t); }
  }
  bar_lgkm();

  if (w < 4) {
    f32x4 cs[2][4]; float mu[2], rstd[2];
#pragma unroll
    for (int b = 0; b < 2; ++b)
#pragma unroll
      for (int g = 0; g < 4; ++g) cs[b][g] = *(LAS f32x4*)(lds + U_CS + (8 * b + 2 * g + h) * 16);
#pragma unroll
    for (int a = 0; a < 2; ++a) {
      auto r = __builtin_amdgcn_permlane32_swap(__float_as_uint(acc[a][1][12]), __float_as_uint(acc[a][1][12]), false, false);
      ln_scalars(__uint_as_float(r[1]), sqv[a], mu[a], rstd[a]); }
    SBAR();
#pragma unroll
    for (int b = 0; b < 2; ++b)
#pragma unroll
      for (int g = 0; g < 4; ++g) {
        const int qi = 8 * b + 2 * g + h, mat = (qi >= 5) + (qi >= 10), pos = qi - 5 * mat;
        const float sc = mat == 0 ? QSCALE : 1.0f;
        int off, tstr;
        if (!BL) {
          const int qo = UA_Q + 2 * w * 1536 + (pos >> 1) * 512 + l31 * 16 + (pos & 1) * 8;
          const int kb = RS0 + (w >> 1) * KVSTAGE + 2 * (w & 1) * 1536;
          const int ko = kb + KVK + (pos >> 1) * 512 + l31 * 16 + (pos & 1) * 8;
          const int vo = kb + KVV + pos * 256 + (((l31 >> 2) ^ pos) * 32) + (l31 & 3) * 8;
          off = mat == 0 ? qo : (mat == 1 ? ko : vo); tstr = 1536;
        } else {
          const int par = l31 & 1, idx = 8 + 32 * w + (l31 >> 1);
          const int qk = (mat ? UB_K : UB_Q) + par * 6912 + (pos >> 1) * 2304 + idx * 16 + (pos & 1) * 8;
          const int vo = UB_V + par * 7296 + pos * 1216 + idx * 8;
          off = mat == 2 ? vo : qk; tstr = mat == 2 ? 128 : 256;
        }
        if (qi == 15) { off = U_DUMMY + lane * 8; tstr = 0; }
#pragma unroll
        for (int a = 0; a < 2; ++a) {
          const float r = rstd[a] * sc, rm = -r * mu[a];
          const u32x2 o2 = {pkbf(__builtin_fmaf(r, acc[a][b][4 * g], rm * cs[b][g][0]), __builtin_fmaf(r, acc[a][b][4 * g + 1], rm * cs[b][g][1])),
                            pkbf(__builtin_fmaf(r, acc[a][b][4 * g + 2], rm * cs[b][g][2]), __builtin_fmaf(r, acc[a][b][4 * g + 3], rm * cs[b][g][3]))};
          *(LAS u32x2*)(lds + off + a * tstr) = o2;
        }
      }
  } else if (BL) {
    const int lw = w - 4, g = lane >> 4, tt = lane & 15;
    float mu, rstd; ln_scalars(*(LAS float*)(lds + U_SXH + tt * 4), sqv[0], mu, rstd);
    const int qi = 4 * lw + g, mat = (qi >= 5) + (qi >= 10), pos = qi - 5 * mat, par = tt & 1, idx = tt >> 1;
    const f32x4 cs = *(LAS f32x4*)(lds + U_CS + qi * 16);
    int off = mat == 2 ? UB_V + par * 7296 + pos * 1216 + idx * 8 : UB_K + par * 6912 + (pos >> 1) * 2304 + idx * 16 + (pos & 1) * 8;
    if (qi < 5 || qi == 15) off = U_DUMMY + lane * 8;
    *(LAS u32x2*)(lds + off) = (u32x2){pkbf(rstd * (hacc[0] - mu * cs[0]), rstd * (hacc[1] - mu * cs[1])), pkbf(rstd * (hacc[2] - mu * cs[2]), rstd * (hacc[3] - mu * cs[3]))};
    const int pr = lw & 1;
    for (int i = lane; i < 144; i += 64) *(LAS u32x2*)(lds + (lw < 2 ? UB_Q : UB_K) + pr * 6912 + 4608 + i * 16 + 8) = (u32x2){0u, 0u};
    if (lw < 2) for (int i = lane; i < 152; i += 64) *(LAS u32x2*)(lds + UB_V + pr * 7296 + 5 * 1216 + i * 8) = (u32x2){0x3f80u, 0u};
  } else {
    const int lw = w - 4;
#pragma unroll
    for (int i = 0; i < 2; ++i) { const int tl = 2 * (lw & 1) + i, st = lw >> 1;
      if (h == 0) *(LAS u32x2*)(lds + RS0 + st * KVSTAGE + KVK + tl * 1536 + 1024 + l31 * 16 + 8) = (u32x2){0x3f80u, 0u};
      else *(LAS u32x2*)(lds + RS0 + st * KVSTAGE + KVV + tl * 1536 + 5 * 256 + l31 * 8) = (u32x2){0x3f80u, 0u}; }
  }
  bar_lgkm();

  if (!BL) {
    u32x4 cv[3];
#pragma unroll
    for (int i = 0; i < 3; ++i) cv[i] = *(LAS u32x4*)(lds + RS0 + (w * 3 + i) * 1024 + lane * 16);
#pragma unroll
    for (int i = 0; i < 3; ++i) __builtin_amdgcn_raw_buffer_store_b128(cv[i], rs, kvoff + (head * 16 + 2 * x) * KVSTAGE + (w * 3 + i) * 1024 + lane * 16, 0, SC1);

  } else {
    const int G = w, c = lane & 15, g = lane >> 4, q4 = c >> 2, p4 = lane & 3;
    const int iB = 8 + 16 * G, iA = iB - 16, ch = g < 3 ? g : 2, qd1 = p4 == 0 ? 4 : 5;
    const int ia = iA + c < 0 ? 0 : iA + c, ka = iA + 4 * g + q4 < 0 ? 0 : iA + 4 * g + q4, kb = iB + 4 * g + q4;
    bf16x8 qf[2], kA[2], kB[2], v0[2], v1[2];
#pragma unroll
    for (int par = 0; par < 2; ++par) {
      qf[par] = lds_b128(lds + UB_Q + par * 6912 + ch * 2304 + (iB + c) * 16);
      kA[par] = lds_b128(lds + UB_K + par * 6912 + ch * 2304 + ia * 16); kB[par] = lds_b128(lds + UB_K + par * 6912 + ch * 2304 + (iB + c) * 16);
      const lchar* vb = lds + UB_V + par * 7296;
      v0[par] = cat(lds_tr(vb + p4 * 1216 + ka * 8), lds_tr(vb + p4 * 1216 + kb * 8));
      v1[par] = cat(lds_tr(vb + qd1 * 1216 + ka * 8), lds_tr(vb + qd1 * 1216 + kb * 8));
    }
    f32x4 bA, bB;
#pragma unroll
    for (int i = 0; i < 4; ++i) { const int r = 4 * g + i;
      bB[i] = (r <= c && r >= c - 7) ? 0.f : -1e30f;
      bA[i] = (r >= c + 9 && !(x == 0 && G == 0)) ? 0.f : -1e30f; }
    SBAR();
    f32x4 sA[2], sB[2]; float mx[2];
#pragma unroll
    for (int par = 0; par < 2; ++par) { if (g == 3) qf[par] = (bf16x8){0, 0, 0, 0, 0, 0, 0, 0};
      sA[par] = MFMA16(kA[par], qf[par], bA); sB[par] = MFMA16(kB[par], qf[par], bB); }
#pragma unroll
    for (int par = 0; par < 2; ++par)
      mx[par] = __builtin_fmaxf(__builtin_fmaxf(__builtin_fmaxf(sA[par][0], sA[par][1]), __builtin_fmaxf(sA[par][2], sA[par][3])), __builtin_fmaxf(__builtin_fmaxf(sB[par][0], sB[par][1]), __builtin_fmaxf(sB[par][2], sB[par][3])));
#pragma unroll
    for (int par = 0; par < 2; ++par) mx[par] = swap16_max(mx[par]);
#pragma unroll
    for (int par = 0; par < 2; ++par) mx[par] = swap32_max(mx[par]);
    f32x4 t0[2], t1[2];
#pragma unroll
    for (int par = 0; par < 2; ++par) { const float m = mx[par];
      const u32x4 pw = {pkbf(ex2(sA[par][0] - m), ex2(sA[par][1] - m)), pkbf(ex2(sA[par][2] - m), ex2(sA[par][3] - m)), pkbf(ex2(sB[par][0] - m), ex2(sB[par][1] - m)), pkbf(ex2(sB[par][2] - m), ex2(sB[par][3] - m))};
      const bf16x8 pf = __builtin_bit_cast(bf16x8, pw); const f32x4 z = {0.f, 0.f, 0.f, 0.f};
      t0[par] = MFMA16(v0[par], pf, z); t1[par] = MFMA16(v1[par], pf, z); }
    u32x2 o0[2], o1[2];
#pragma unroll
    for (int par = 0; par < 2; ++par) {
      auto r1 = __builtin_amdgcn_permlane16_swap(__float_as_uint(t1[par][0]), __float_as_uint(t1[par][0]), false, false);
      auto r2 = __builtin_amdgcn_permlane32_swap(r1[1], r1[1], false, false);
      const float inv = __builtin_amdgcn_rcpf(__uint_as_float(r2[0]));
      o0[par] = (u32x2){pkbf(t0[par][0] * inv, t0[par][1] * inv), pkbf(t0[par][2] * inv, t0[par][3] * inv)};
      o1[par] = (u32x2){pkbf(t1[par][0] * inv, t1[par][1] * inv), pkbf(t1[par][2] * inv, t1[par][3] * inv)};
    }
    const int cA = (5 * head + 1) >> 1, odd = head & 1;
    const unsigned ob = ooff + (8 * x + G) * 40960 + (2 * c + (g & 1)) * 16;
    auto rx = __builtin_amdgcn_permlane16_swap(o0[0][0], o0[1][0], false, false), ry = __builtin_amdgcn_permlane16_swap(o0[0][1], o0[1][1], false, false);
    __builtin_amdgcn_raw_buffer_store_b128((u32x4){rx[0], ry[0], rx[1], ry[1]}, rs, ob + (cA + (g >> 1)) * 512, 0, SC1);
    auto sx2 = __builtin_amdgcn_permlane16_swap(o1[0][0], o1[1][0], false, false), sy2 = __builtin_amdgcn_permlane16_swap(o1[0][1], o1[1][1], false, false);
    if (g < 2) __builtin_amdgcn_raw_buffer_store_b64((u32x2){sx2[0], sy2[0]}, rs, ob + (odd ? cA - 1 : cA + 2) * 512 + odd * 8, 0, SC1);

    bar_vm_lgkm();
    if (tid == 0) flag_set(sy, PH, x * 32 + head);
    proj_w_issue(lds, nextw, head >> 2, w, lane, 0, 25);
  }
}

DI int rslot(int s) { return RS0 - s * ROUND; }
template <int PH, bool FUSED>
DI void phase_attn(lchar* lds, const Params& p, const Sync& sy, unsigned qoff, unsigned kvoff, unsigned ooff, const char* nextw, const char* touchw, int x, int head) {
  const unsigned lds0 = (unsigned)(uintptr_t)lds;
  const int tid = threadIdx.x, lane = tid & 63, w = __builtin_amdgcn_readfirstlane(tid >> 6);
  const int h = lane >> 5, l31 = lane & 31;
  const int qg = w & 3, kh = w >> 2, hp = head >> 1;
  rsrc_t rs = __builtin_amdgcn_make_buffer_rsrc((void*)p.ws, 0, WS_END, 0x00020000);
  const char* kvbase = p.ws + kvoff + (size_t)head * 16 * KVSTAGE;
  const unsigned dvoff = lane * 16;
  auto issue = [&](int r, int slot) { dma3(dvoff, kvbase + w * 3072 + ((x + r) & 7) * ROUND, lds0 + rslot(slot) + w * 3072); };
  if (!FUSED) flag_wait(sy, PH - 1, hp * 16 + 2 * x, 2, lane);

  const int tb = 8 * x + 2 * qg;
  bf16x8 qA0, qB0; u32x2 qa1, qb1;
  if (FUSED) {
    qA0 = lds_b128(lds + UA_Q + 2 * qg * 1536 + h * 512 + l31 * 16); qB0 = lds_b128(lds + UA_Q + (2 * qg + 1) * 1536 + h * 512 + l31 * 16);
    qa1 = *(LAS u32x2*)(lds + UA_Q + 2 * qg * 1536 + 1024 + l31 * 16); qb1 = *(LAS u32x2*)(lds + UA_Q + (2 * qg + 1) * 1536 + 1024 + l31 * 16);
  } else {
    const unsigned qb = qoff + (head * 64 + tb) * 1536;
    qA0 = __builtin_bit_cast(bf16x8, __builtin_amdgcn_raw_buffer_load_b128(rs, qb + h * 512 + l31 * 16, 0, SC1));
    qB0 = __builtin_bit_cast(bf16x8, __builtin_amdgcn_raw_buffer_load_b128(rs, qb + 1536 + h * 512 + l31 * 16, 0, SC1));
    qa1 = __builtin_amdgcn_raw_buffer_load_b64(rs, qb + 1024 + l31 * 16, 0, SC1);
    qb1 = __builtin_amdgcn_raw_buffer_load_b64(rs, qb + 1536 + 1024 + l31 * 16, 0, SC1);
  }
  const int dg = (lane >> 4) & 1, qq = (lane & 15) >> 2, p4 = lane & 3;
  const int qd = dg ? (p4 == 0 ? 4 : 5) : p4;
  const f32x16 zero = {};
  s16x4 qA1, qB1;
  float o[12];
  for (int attempt = 0;; ++attempt) {
    if (!FUSED || attempt) {
      issue(0, 0);
      asm volatile("s_waitcnt vmcnt(0)\n\ts_barrier" ::: "memory");
    }

    const lchar* kp0 = lds + RS0 + kh * KVSTAGE + KVK + h * 512 + l31 * 16;
    const lchar* kp1 = lds + RS0 + kh * KVSTAGE + KVK + 1024 + l31 * 16 + h * 8;
    const lchar* vp00 = lds + RS0 + kh * KVSTAGE + KVV + qd * 256 + (((0 + h) ^ qd) * 32) + qq * 8;
    const lchar* vp01 = lds + RS0 + kh * KVSTAGE + KVV + qd * 256 + (((2 + h) ^ qd) * 32) + qq * 8;
    const lchar* vp10 = lds + RS0 + kh * KVSTAGE + KVV + qd * 256 + (((4 + h) ^ qd) * 32) + qq * 8;
    const lchar* vp11 = lds + RS0 + kh * KVSTAGE + KVV + qd * 256 + (((6 + h) ^ qd) * 32) + qq * 8;
    {
      const u32x2 ta = {h ? 0u : qa1[0], h ? 0u : qa1[1]}, tb2 = {h ? 0u : qb1[0], h ? 0u : qb1[1]};
      float ma, mb;
      if (attempt == 0) {
        const bf16x8 a0 = lds_b128(lds + RS0 + KVK + h * 512 + l31 * 16);
        const s16x4 a1 = lds_b64(lds + RS0 + KVK + 1024 + l31 * 16 + h * 8);
        f32x16 sa = MFMA32(a0, qA0, zero); sa = MFMA8(a1, __builtin_bit_cast(s16x4, ta), sa);
        f32x16 sb = MFMA32(a0, qB0, zero); sb = MFMA8(a1, __builtin_bit_cast(s16x4, tb2), sb);
        ma = sa[0]; mb = sb[0];
#pragma unroll
        for (int r = 1; r < 16; ++r) { ma = __builtin_fmaxf(ma, sa[r]); mb = __builtin_fmaxf(mb, sb[r]); }
      } else {
        ma = -INFINITY; mb = -INFINITY;
#pragma unroll 1
        for (int t = 0; t < 64; ++t) {
          const unsigned kb = kvoff + (head * 16 + (t >> 2)) * KVSTAGE + KVK + (t & 3) * 1536;
          const bf16x8 a0 = __builtin_bit_cast(bf16x8, __builtin_amdgcn_raw_buffer_load_b128(rs, kb + h * 512 + l31 * 16, 0, SC1));
          const s16x4 a1 = __builtin_bit_cast(s16x4, __builtin_amdgcn_raw_buffer_load_b64(rs, kb + 1024 + l31 * 16 + h * 8, 0, SC1));
          f32x16 sa = MFMA32(a0, qA0, zero); sa = MFMA8(a1, __builtin_bit_cast(s16x4, ta), sa);
          f32x16 sb = MFMA32(a0, qB0, zero); sb = MFMA8(a1, __builtin_bit_cast(s16x4, tb2), sb);
#pragma unroll
          for (int r = 0; r < 16; ++r) { ma = __builtin_fmaxf(ma, sa[r]); mb = __builtin_fmaxf(mb, sb[r]); }
        }
      }
      ma = swap32_max(ma); mb = swap32_max(mb);
      const unsigned na = pkbf(-ma, 0.f), nb = pkbf(-mb, 0.f);
      const u32x2 ua = {h ? na : qa1[0], h ? 0u : qa1[1]}, ub = {h ? nb : qb1[0], h ? 0u : qb1[1]};
      qA1 = __builtin_bit_cast(s16x4, ua); qB1 = __builtin_bit_cast(s16x4, ub);
    }
    f32x16 oA = {}, oB = {}, sA, sB;
    u32x4 pA0 = {}, pA1 = {}, pB0 = {}, pB1 = {};
    bf16x8 kf0, vf0, vf1; s16x4 kf1;
#define KRD(P0, P1, TL) do { kf0 = lds_b128((P0) + (TL) * 1536); kf1 = lds_b64((P1) + (TL) * 1536); } while (0)
#define VRD(TL) do { vf0 = cat(lds_tr(vp00 + (TL) * 1536), lds_tr(vp01 + (TL) * 1536)); vf1 = cat(lds_tr(vp10 + (TL) * 1536), lds_tr(vp11 + (TL) * 1536)); } while (0)
    KRD(kp0, kp1, 0); VRD(0);
    sA = MFMA32(kf0, qA0, zero); sA = MFMA8(kf1, qA1, sA);
#define SP(Y, S, g) do { if (Y) { if ((S) == 1 && (g) == 3) __builtin_amdgcn_s_setprio(0); if ((S) == 0 && (g) == 0) __builtin_amdgcn_s_setprio(1); } } while (0)
#define HSTEP(Y, S, SC, PC0, PC1, SN, QN0, QN1, ON, PN0, PN1, LOADK, LOADV) do { \
    SBAR(); SP(Y, S, 0); \
    SN = MFMA32(kf0, QN0, zero); SBAR(); \
    SC[0] = ex2(SC[0]); SC[1] = ex2(SC[1]); SC[2] = ex2(SC[2]); SC[3] = ex2(SC[3]); PIN(SC); SBAR(); SP(Y, S, 1); \
    SN = MFMA8(kf1, QN1, SN); SBAR(); \
    LOADK; \
    SC[4] = ex2(SC[4]); SC[5] = ex2(SC[5]); SC[6] = ex2(SC[6]); SC[7] = ex2(SC[7]); \
    PC0[0] = pkbf(SC[0], SC[1]); PC0[1] = pkbf(SC[2], SC[3]); PIN(SC); PIN(PC0); SBAR(); SP(Y, S, 2); \
    ON = MFMA32(vf0, __builtin_bit_cast(bf16x8, PN0), ON); SBAR(); \
    SC[8] = ex2(SC[8]); SC[9] = ex2(SC[9]); SC[10] = ex2(SC[10]); SC[11] = ex2(SC[11]); \
    PC0[2] = pkbf(SC[4], SC[5]); PC0[3] = pkbf(SC[6], SC[7]); PIN(SC); PIN(PC0); SBAR(); SP(Y, S, 3); \
    ON = MFMA32(vf1, __builtin_bit_cast(bf16x8, PN1), ON); SBAR(); \
    LOADV; \
    SC[12] = ex2(SC[12]); SC[13] = ex2(SC[13]); SC[14] = ex2(SC[14]); SC[15] = ex2(SC[15]); \
    PC1[0] = pkbf(SC[8], SC[9]); PC1[1] = pkbf(SC[10], SC[11]); \
    PC1[2] = pkbf(SC[12], SC[13]); PC1[3] = pkbf(SC[14], SC[15]); PIN(PC1); SBAR(); \
  } while (0)
#define NOP_ do {} while (0)
#define XSTEP(Y, LK, LV) HSTEP(Y, 0, sA, pA0, pA1, sB, qB0, qB1, oB, pB0, pB1, LK, LV)
#define YSTEP(Y) HSTEP(Y, 1, sB, pB0, pB1, sA, qA0, qA1, oA, pA0, pA1, NOP_, NOP_)
    int slot = 0;
    const char* wsl = nextw + (size_t)(head >> 2) * 102400;
    auto refill = [&](int r, int sl2) {
      if (r < 6) issue(r + 2, sl2);
      const int gi = r == 6 ? 21 + w : 8 * (r - 1) + w;
      if (((r >= 1 && r <= 3 && gi < 21) || (r == 6 && gi < 25))) dma4(dvoff, wsl + gi * 4096, lds0 + gi * 4096);
      const unsigned dd = (r < 6 ? lds0 + 86016 : lds0 + 102400) + w * 256;
      if (r >= 2 && r <= 6 && w < 2) l2_touch(touchw + (size_t)head * 81920, dd, 2 * (r - 2) + w, lane);
    };
#define ROUND_(Y, MID) do { \
      const int nslot = slot == 2 ? 0 : slot + 1, n2slot = slot == 0 ? 2 : slot - 1; \
      const int dn = rslot(nslot) - rslot(slot); \
      const lchar* kn0 = kp0 + dn; const lchar* kn1 = kp1 + dn; \
      XSTEP(Y, KRD(kp0, kp1, 1), VRD(0)); YSTEP(Y); \
      MID; \
      XSTEP(Y, KRD(kp0, kp1, 2), VRD(1)); YSTEP(Y); \
      XSTEP(Y, KRD(kp0, kp1, 3), VRD(2)); YSTEP(Y); \
      asm volatile("s_waitcnt vmcnt(0)\n\ts_barrier" ::: "memory"); \
      if (!Y) refill(r, n2slot); \
      XSTEP(Y, KRD(kn0, kn1, 0), VRD(3)); YSTEP(Y); \
      kp0 = kn0; kp1 = kn1; vp00 += dn; vp01 += dn; vp10 += dn; vp11 += dn; slot = nslot; } while (0)
#define MID0 do { \
        if (FUSED) { if (attempt == 0) {     \
            asm volatile("s_waitcnt vmcnt(0)\n\ts_barrier" ::: "memory"); \
            if (tid == 0) flag_set(sy, PH - 1, head * 8 + x); \
            if (w == 0) flag_wait(sy, PH - 1, head * 8, 8, lane); } \
        } else { \
          if (attempt == 0 && w == 0) flag_wait(sy, PH - 1, hp * 16, 16, lane); \
          if (attempt == 0 && PH == 1 && w == 1) { flag_wait(sy, 0, 0, 64, lane); flag_wait(sy, 0, 64, 64, lane); flag_wait(sy, 0, 128, 64, lane); flag_wait(sy, 0, 192, 64, lane); } } \
        bar(); issue(1, 1); } while (0)
#define ROUNDS(Y) \
    { const int r = 0; ROUND_(Y, MID0); } \
    for (int r = 1; r < 8; ++r) ROUND_(Y, if (Y) refill(r - 1, nslot));
    if (w >= 4) { ROUNDS(true) __builtin_amdgcn_s_setprio(0); } else { ROUNDS(false) }
    oB = MFMA32(vf0, __builtin_bit_cast(bf16x8, pB0), oB);
    oB = MFMA32(vf1, __builtin_bit_cast(bf16x8, pB1), oB);

    bar_lgkm();
    LAS f32x4* xb = (LAS f32x4*)(lds + RS1) + (qg * 2) * 192 + lane;
    if (kh) { xb[0] = (f32x4){oA[0], oA[1], oA[2], oA[3]}; xb[64] = (f32x4){oA[4], oA[5], oA[6], oA[7]}; xb[128] = (f32x4){oA[8], oA[9], oA[10], oA[11]}; }
    else { xb[192] = (f32x4){oB[0], oB[1], oB[2], oB[3]}; xb[192 + 64] = (f32x4){oB[4], oB[5], oB[6], oB[7]}; xb[192 + 128] = (f32x4){oB[8], oB[9], oB[10], oB[11]}; }
    if (tid == 0) *(LAS unsigned*)(lds + RS0) = 0u;
    bar_lgkm();
    const int sel = kh ? 192 : 0;
    const f32x4 a = xb[sel], b = xb[sel + 64], c = xb[sel + 128];
#pragma unroll
    for (int i = 0; i < 4; ++i) { o[i] = (kh ? oB[i] : oA[i]) + a[i]; o[4 + i] = (kh ? oB[4 + i] : oA[4 + i]) + b[i]; o[8 + i] = (kh ? oB[8 + i] : oA[8 + i]) + c[i]; }
    auto rr = __builtin_amdgcn_permlane32_swap(__float_as_uint(o[8]), __float_as_uint(o[8]), false, false);
    const float l = __uint_as_float(rr[1]);
    const float inv = 1.0f / l;
#pragma unroll
    for (int i = 0; i < 12; ++i) o[i] *= inv;
    const int cA = (5 * head + 1) >> 1, odd = head & 1;
    const unsigned ob = ooff + (tb + kh) * 40960 + l31 * 16;
    {
      auto sx_ = __builtin_amdgcn_permlane32_swap(pkbf(o[0], o[1]), pkbf(o[4], o[5]), false, false), sy_ = __builtin_amdgcn_permlane32_swap(pkbf(o[2], o[3]), pkbf(o[6], o[7]), false, false);
      __builtin_amdgcn_raw_buffer_store_b128((u32x4){sx_[0], sy_[0], sx_[1], sy_[1]}, rs, ob + (cA + h) * 512, 0, SC1); }
    if (h == 0) __builtin_amdgcn_raw_buffer_store_b64((u32x2){pkbf(o[8], o[9]), pkbf(o[10], o[11])}, rs, ob + (odd ? cA - 1 : cA + 2) * 512 + odd * 8, 0, SC1);
    const bool bad = !(l >= 8.6736174e-19f && l <= 1.1529215e18f);
    if (attempt == 0 && __any(bad) && lane == 0) *(LAS unsigned*)(lds + RS0) = 1u;
    bar_vm_lgkm();
    const unsigned redo = __builtin_amdgcn_readfirstlane(*(LAS unsigned*)(lds + RS0));
    if (!redo || attempt == 1) break;
    bar_lgkm();
  }

  if (tid == 0) flag_set(sy, PH, x * 32 + head);
}

constexpr int P_RED = 141312;
template <bool FIRST, bool LAST, int PH>
DI void phase_proj(lchar* lds, const Params& p, const Sync& sy, unsigned ooff, unsigned xin, unsigned xout, unsigned sqout, const char* nextw, int bx, int bj) {
  const int tid = threadIdx.x, lane = tid & 63, w = __builtin_amdgcn_readfirstlane(tid >> 6);
  const int rt = 4 * bx + (bj & 3), ct = bj >> 2;
  const int wm = w & 3, wk = w >> 2, tok16 = lane & 15, g = lane >> 4;
  rsrc_t rs = __builtin_amdgcn_make_buffer_rsrc((void*)p.ws, 0, WS_END, 0x00020000);
  const int token = 64 * rt + 16 * wm + tok16;
  const unsigned xtok = (token >> 5) * 40960 + (token & 31) * 16;
  u32x2 res[3];
#pragma unroll
  for (int j = 0; j < 3; ++j) { const int t = wk ? 3 + (j & 1) : j; const int quad = 20 * ct + 4 * t + g;
    if (FIRST) res[j] = *(const u32x2*)(p.x0 + (size_t)token * 1280 + quad * 8);
    else res[j] = __builtin_amdgcn_raw_buffer_load_b64(rs, xin + xtok + (quad >> 1) * 512 + (quad & 1) * 8, 0, SC1); }
  if (tid == 0) *(volatile LAS unsigned*)(lds + RELAY) = 0u;
  bar_vm_lgkm();
  flag_wait_relay(sy, PH - 1, bx * 32, 32, lane, w == 0, lds + RELAY);

  u32x4 a[10];
  {
    const unsigned av = ooff + xtok + (40 * wk + g) * 512;
    const char* wsb = uni(p.ws);
#define ALD(s) asm volatile("global_load_dwordx4 %0, %1, %2 offset:0 sc1" : "=&v"(a[s]) : "v"(av + (s) * 2048), "s"(wsb) : "memory")
    ALD(0); ALD(1); ALD(2); ALD(3); ALD(4); ALD(5); ALD(6); ALD(7); ALD(8); ALD(9);
#undef ALD
  }

  f32x4 acc[5];
#pragma unroll
  for (int t = 0; t < 5; ++t) acc[t] = (f32x4){0.f, 0.f, 0.f, 0.f};
  const int q4 = (lane & 15) >> 2, p4 = lane & 3;
  const lchar* wb = lds + (320 * wk + 16 * (g >> 1) + 4 * (g & 1) + q4) * 160 + p4 * 8;
  bf16x8 pwf[2][5];
#define PWRD(s) do { _Pragma("unroll") for (int t = 0; t < 5; ++t) pwf[(s) & 1][t] = cat(lds_tr(wb + (s) * 5120 + t * 32), lds_tr(wb + (s) * 5120 + 1280 + t * 32)); } while (0)
#define PSTEP(s) do { if ((s) < 9) PWRD((s) + 1); SBAR(); asm volatile("s_waitcnt vmcnt(%1)" : "+v"(a[s]) : "i"(9 - (s) + ((LAST || (s) < 5) ? 0 : 6))); SBAR(); \
    const bf16x8 bf = __builtin_bit_cast(bf16x8, a[s]); \
    _Pragma("unroll") for (int t = 0; t < 5; ++t) acc[t] = MFMA16(pwf[(s) & 1][t], bf, acc[t]); SBAR(); } while (0)
  PWRD(0);
  PSTEP(0); PSTEP(1); PSTEP(2); PSTEP(3); PSTEP(4);
  if (!LAST) qkv_prefetch(lds, nextw, bj, 0);
  PSTEP(5); PSTEP(6); PSTEP(7); PSTEP(8); PSTEP(9);
#undef PWRD
#undef PSTEP

  LAS f32x4* red = (LAS f32x4*)(lds + P_RED) + wm * 320 + lane;
  if (wk) { red[0] = acc[0]; red[64] = acc[1]; red[128] = acc[2]; } else { red[192] = acc[3]; red[256] = acc[4]; }
  bar_lgkm();

  float sq = 0.f;
  u32x2 pk[3];
#pragma unroll
  for (int j = 0; j < 3; ++j) {
    const int t = wk ? 3 + (j & 1) : j;
    const f32x4 o = red[t * 64];
    const f32x4 me = wk ? ((j & 1) ? acc[4] : acc[3]) : acc[j];
    pk[j] = (u32x2){pkbf(me[0] + o[0] + bflo(res[j][0]), me[1] + o[1] + bfhi(res[j][0])), pkbf(me[2] + o[2] + bflo(res[j][1]), me[3] + o[3] + bfhi(res[j][1]))};
    if (j < 2 || !wk) { const float r0 = bflo(pk[j][0]), r1 = bfhi(pk[j][0]), r2 = bflo(pk[j][1]), r3 = bfhi(pk[j][1]);
      sq += (r0 * r0 + r1 * r1) + (r2 * r2 + r3 * r3); }
  }
  if (LAST) {
#pragma unroll
    for (int j = 0; j < 3; ++j) { if (wk && j == 2) break; *(u32x2*)(p.out + (size_t)token * 1280 + (20 * ct + 4 * (wk ? 3 + j : j) + g) * 8) = pk[j]; }
  } else {
    auto sx_ = __builtin_amdgcn_permlane16_swap(pk[0][0], pk[1][0], false, false), sy_ = __builtin_amdgcn_permlane16_swap(pk[0][1], pk[1][1], false, false);
    __builtin_amdgcn_raw_buffer_store_b128((u32x4){sx_[0], sy_[0], sx_[1], sy_[1]}, rs, xout + xtok + (10 * ct + 2 * ((wk ? 3 : 0) + (g & 1)) + (g >> 1)) * 512, 0, SC1);
    if (!wk) __builtin_amdgcn_raw_buffer_store_b64(pk[2], rs, xout + xtok + (10 * ct + 4 + (g >> 1)) * 512 + (g & 1) * 8, 0, SC1);
    sq = swap32_sum(swap16_sum(sq));
    if (g == 0) __builtin_amdgcn_raw_buffer_store_b32(__float_as_uint(sq), rs, sqout + (token >> 5) * 2048 + (2 * ct + wk) * 128 + (token & 31) * 4, 0, SC1);
  }

  if (!LAST) {
    if (tid == 0) *(volatile LAS unsigned*)(lds + RELAY) = 0u;
    if (tid >= 64 && tid < 68) *(volatile LAS unsigned*)(lds + U_FLG + (tid - 64) * 4) = 0u;
    bar_vm_lgkm();
    if (tid == 0) flag_set(sy, PH, rt * 8 + ct);
    qkv_prefetch(lds, nextw, bj, 1);
  }
}

__global__ void __launch_bounds__(512, 2) k_stack(Params p) {
  __shared__ __attribute__((aligned(16))) char smem[163840];
  lchar* lds = (lchar*)smem;
  const int bx = blockIdx.x & 7, bj = blockIdx.x >> 3;
  Sync sy; sy.ws = p.ws;
  sy.nonce = __hip_atomic_load((const GAS u64*)(p.ws + WS_NONCE), __ATOMIC_RELAXED, __HIP_MEMORY_SCOPE_AGENT);
  { u64 pc; int len;
    asm volatile("s_getpc_b64 %0\n.Lstack_here:\n\ts_mov_b32 %1, .Lstack_tail-.Lstack_here" : "=s"(pc), "=s"(len));
    sy.code = (const char*)pc; sy.clen = len; }
  if (threadIdx.x >= 448) code_touch(sy, (unsigned)(uintptr_t)lds + TOUCH0, bj, threadIdx.x & 63);
  const char* PWA = p.ws + WS_PW; const char* PWB_ = p.ws + WS_PW + PWB; const char* POA = p.ws + WS_PO; const char* POBp = p.ws + WS_PO + POB_;
  phase_first(lds, p, sy, bx, bj);
  phase_attn<1, false>(lds, p, sy, WS_Q, WS_KV, WS_O, POA, PWB_, bx, bj);
  phase_proj<true, false, 2>(lds, p, sy, WS_O, 0, WS_X, WS_SQ, PWB_, bx, bj);
  phase_qkv2<true, 3>(lds, p, sy, WS_X, WS_SQ, 0, WS_O + XB, POBp, PWA, bx, bj);
  phase_proj<false, false, 4>(lds, p, sy, WS_O + XB, WS_X, WS_X + XB, WS_SQ + SQB, PWA, bx, bj);
  phase_qkv2<false, 5>(lds, p, sy, WS_X + XB, WS_SQ + SQB, WS_KV + KVBYTES, 0, nullptr, nullptr, bx, bj);
  phase_attn<6, true>(lds, p, sy, 0, WS_KV + KVBYTES, WS_O + 2 * XB, POA, PWB_, bx, bj);
  phase_proj<false, false, 7>(lds, p, sy, WS_O + 2 * XB, WS_X + XB, WS_X + 2 * XB, WS_SQ + 2 * SQB, PWB_, bx, bj);
  phase_qkv2<true, 8>(lds, p, sy, WS_X + 2 * XB, WS_SQ + 2 * SQB, 0, WS_O + 3 * XB, POBp, nullptr, bx, bj);
  phase_proj<false, true, 9>(lds, p, sy, WS_O + 3 * XB, WS_X + 2 * XB, 0, 0, nullptr, bx, bj);
  asm volatile(".Lstack_tail:");
  if (blockIdx.x == 0 && threadIdx.x == 0) __hip_atomic_store((GAS u64*)(p.ws + WS_NONCE), sy.nonce + 16, __ATOMIC_RELAXED, __HIP_MEMORY_SCOPE_AGENT);
}

extern "C" void kernel_launch(void* const* d_in, const int* in_sizes, int n_in, void* d_out, int out_size, void* d_ws, size_t ws_size, hipStream_t stream) {
  Params p;
  p.x0 = (const char*)d_in[0]; p.wqA = (const char*)d_in[1]; p.woA = (const char*)d_in[2];
  p.wqB = (const char*)d_in[4]; p.woB = (const char*)d_in[5];
  p.out = (char*)d_out; p.ws = (char*)d_ws;
  k_stack<<<256, 512, 0, stream>>>(p);
}
```

```cpp
#include <hip/hip_runtime.h>
#include <stdint.h>

#define DI __device__ __forceinline__
typedef short bf16x8 __attribute__((ext_vector_type(8)));
typedef short s16x4 __attribute__((ext_vector_type(4)));
typedef short v4i16_t __attribute__((ext_vector_type(4)));
typedef float f32x16 __attribute__((ext_vector_type(16)));
typedef float f32x4 __attribute__((ext_vector_type(4)));
typedef unsigned u32x2 __attribute__((ext_vector_type(2)));
typedef unsigned u32x4 __attribute__((ext_vector_type(4)));
typedef unsigned long long u64;
typedef __bf16 bf16x2_t __attribute__((ext_vector_type(2)));
#define LAS __attribute__((address_space(3)))
#define GAS __attribute__((address_space(1)))
typedef LAS char lchar;
typedef __amdgpu_buffer_rsrc_t rsrc_t;
#define SBAR() __builtin_amdgcn_sched_barrier(0)
#define PIN(x) asm volatile("" : "+v"(x))
#define MFMA32(a, b, c) __builtin_amdgcn_mfma_f32_32x32x16_bf16(a, b, c, 0, 0, 0)
#define MFMA8(a, b, c) __builtin_amdgcn_mfma_f32_32x32x8bf16_1k(a, b, c, 0, 0, 0)
#define MFMA16(a, b, c) __builtin_amdgcn_mfma_f32_16x16x32_bf16(a, b, c, 0, 0, 0)
#define SC1 16

DI unsigned pkbf(float a, float b) { bf16x2_t v = {(__bf16)a, (__bf16)b}; return __builtin_bit_cast(unsigned, v); }
DI float bflo(unsigned u) { return __uint_as_float(u << 16); }
DI float bfhi(unsigned u) { return __uint_as_float(u & 0xffff0000u); }
DI float ex2(float x) { return __builtin_amdgcn_exp2f(x); }
DI float rbf(float x) { return __uint_as_float(pkbf(x, 0.f) << 16); }
DI void ln_scalars(float sx, float sq, float& mu, float& rstd) {
  const float m = sx * (1.0f / 640.0f), var = __builtin_fmaxf(sq * (1.0f / 640.0f) - m * m, 0.f);
  mu = rbf(m);
  rstd = __builtin_amdgcn_rcpf(rbf(__builtin_amdgcn_sqrtf(rbf(rbf(var) + 1e-5f))));
}
DI bf16x8 lds_b128(const lchar* p) { return *(const LAS bf16x8*)p; }
DI s16x4 lds_b64(const lchar* p) { return *(const LAS s16x4*)p; }
DI s16x4 lds_tr(const lchar* p) { return __builtin_bit_cast(s16x4, __builtin_amdgcn_ds_read_tr16_b64_v4i16((LAS v4i16_t*)p)); }
DI bf16x8 cat(s16x4 a, s16x4 b) { return __builtin_shufflevector(a, b, 0, 1, 2, 3, 4, 5, 6, 7); }
DI float swap32_sum(float v) { auto r = __builtin_amdgcn_permlane32_swap(__float_as_uint(v), __float_as_uint(v), false, false); return __uint_as_float(r[0]) + __uint_as_float(r[1]); }
DI float swap32_max(float v) { auto r = __builtin_amdgcn_permlane32_swap(__float_as_uint(v), __float_as_uint(v), false, false); return __builtin_fmaxf(__uint_as_float(r[0]), __uint_as_float(r[1])); }
DI float swap16_sum(float v) { auto r = __builtin_amdgcn_permlane16_swap(__float_as_uint(v), __float_as_uint(v), false, false); return __uint_as_float(r[0]) + __uint_as_float(r[1]); }
DI float swap16_max(float v) { auto r = __builtin_amdgcn_permlane16_swap(__float_as_uint(v), __float_as_uint(v), false, false); return __builtin_fmaxf(__uint_as_float(r[0]), __uint_as_float(r[1])); }

constexpr int S = 2048, D = 640;
constexpr float QSCALE = 0.22360679774997896f * 1.4426950408889634f;
constexpr unsigned XB = S * D * 2;
constexpr int KVSTAGE = 12288;
constexpr int KVK = 0, KVV = 6144, ROUND = 2 * KVSTAGE;
constexpr unsigned QBYTES = 32 * 64 * 1536, KVBYTES = 32 * 16 * KVSTAGE;
constexpr unsigned PWB = 32 * 81920, POB_ = 8 * 102400;
constexpr unsigned WS_FLAG = 0;
constexpr unsigned WS_NONCE = 10 * 256 * 8;
constexpr unsigned WS_X = 32768;
constexpr unsigned WS_O = WS_X + 3 * XB;
constexpr unsigned WS_Q = WS_O + 4 * XB;
constexpr unsigned WS_KV = WS_Q + QBYTES;
constexpr unsigned WS_PW = WS_KV + 2 * KVBYTES;
constexpr unsigned WS_PO = WS_PW + 2 * PWB;
constexpr unsigned WS_SQ = WS_PO + 2 * POB_;
constexpr unsigned SQB = S * 64;
constexpr unsigned WS_END = WS_SQ + 3 * SQB;

struct Params { const char* x0; const char* wqA; const char* woA; const char* wqB; const char* woB; char* out; char* ws; };

DI const char* uni(const char* p) { const uint64_t v = (uint64_t)p; const unsigned lo = __builtin_amdgcn_readfirstlane((unsigned)v), hi = __builtin_amdgcn_readfirstlane((unsigned)(v >> 32)); return (const char*)(((uint64_t)hi << 32) | lo); }
DI unsigned uni(unsigned v) { return __builtin_amdgcn_readfirstlane(v); }
#define GLDS "global_load_lds_dwordx4 "
DI void dma1(unsigned v, const char* sb, unsigned dst) {
  asm volatile("s_mov_b32 m0, %2\n\ts_nop 3\n\t" GLDS "%0, %1 sc1" :: "v"(v), "s"(uni(sb)), "s"(uni(dst)) : "memory"); }
DI void dma2(unsigned v, const char* sb, unsigned dst) {
  asm volatile("s_mov_b32 m0, %2\n\ts_nop 3\n\t" GLDS "%0, %1 sc1\n\t" GLDS "%0, %1 offset:1024 sc1" :: "v"(v), "s"(uni(sb)), "s"(uni(dst)) : "memory"); }
DI void dma3(unsigned v, const char* sb, unsigned dst) {
  asm volatile("s_mov_b32 m0, %2\n\ts_nop 3\n\t" GLDS "%0, %1 sc1\n\t" GLDS "%0, %1 offset:1024 sc1\n\t" GLDS "%0, %1 offset:2048 sc1" :: "v"(v), "s"(uni(sb)), "s"(uni(dst)) : "memory"); }
DI void dma4(unsigned v, const char* sb, unsigned dst) {
  asm volatile("s_mov_b32 m0, %2\n\ts_nop 3\n\t" GLDS "%0, %1 sc1\n\t" GLDS "%0, %1 offset:1024 sc1\n\t" GLDS "%0, %1 offset:2048 sc1\n\t" GLDS "%0, %1 offset:3072 sc1"
               :: "v"(v), "s"(uni(sb)), "s"(uni(dst)) : "memory"); }
DI void dma4v(unsigned v0, unsigned v1, unsigned v2, unsigned v3, const char* sb, unsigned dst) {
  asm volatile("s_mov_b32 m0, %5\n\ts_nop 3\n\t" GLDS "%0, %4\n\t" GLDS "%1, %4 offset:1024\n\t" GLDS "%2, %4 offset:2048\n\t" GLDS "%3, %4 offset:3072"
               :: "v"(v0), "v"(v1), "v"(v2), "v"(v3), "s"(uni(sb)), "s"(uni(dst)) : "memory"); }
DI void dma4vm(unsigned v0, unsigned v1, unsigned v2, unsigned v3, const char* sb, unsigned dst, u64 m0_, u64 m1, u64 m2, u64 m3) {
  asm volatile("s_mov_b32 m0, %5\n\ts_mov_b64 exec, %6\n\ts_nop 2\n\t" GLDS "%0, %4\n\ts_mov_b64 exec, %7\n\t" GLDS "%1, %4 offset:1024\n\ts_mov_b64 exec, %8\n\t" GLDS "%2, %4 offset:2048\n\t"
               "s_mov_b64 exec, %9\n\t" GLDS "%3, %4 offset:3072\n\ts_mov_b64 exec, -1"
               :: "v"(v0), "v"(v1), "v"(v2), "v"(v3), "s"(uni(sb)), "s"(uni(dst)), "s"(m0_), "s"(m1), "s"(m2), "s"(m3) : "memory"); }
DI void l2_touch(const char* base, unsigned dst, int u, int lane) {
  asm volatile("s_mov_b32 m0, %2\n\ts_nop 3\n\tglobal_load_lds_dword %0, %1 sc1" :: "v"(u * 8192 + lane * 128), "s"(uni(base)), "s"(uni(dst)) : "memory"); }
template <int N> DI void wait_vm() { asm volatile("s_waitcnt vmcnt(%0)" :: "i"(N) : "memory"); }
DI void bar() { asm volatile("s_barrier" ::: "memory"); }
DI void bar_lgkm() { asm volatile("s_waitcnt lgkmcnt(0)\n\ts_barrier" ::: "memory"); }
DI void bar_vm_lgkm() { asm volatile("s_waitcnt vmcnt(0) lgkmcnt(0)\n\ts_barrier" ::: "memory"); }

constexpr int RELAY = 161792;
struct Sync { const char* ws; u64 nonce; const char* code; int clen; };
DI u64 tag_of(const Sync& sy, int ph) { return ((((u64)__builtin_amdgcn_readfirstlane((unsigned)(sy.nonce >> 32))) << 32) | __builtin_amdgcn_readfirstlane((unsigned)sy.nonce)) + 1 + ph; }
DI void flag_set(const Sync& sy, int ph, int idx) {
  __hip_atomic_store((GAS u64*)(sy.ws + WS_FLAG + (ph * 256 + idx) * 8), tag_of(sy, ph), __ATOMIC_RELAXED, __HIP_MEMORY_SCOPE_AGENT);
}
DI void code_touch(const Sync& sy, unsigned dst, int bj, int lane) {
  const int o = (bj * 32 + (lane & 31)) * 128, last = sy.clen - 4;
  asm volatile("s_mov_b32 m0, %2\n\ts_nop 3\n\tglobal_load_lds_dword %0, %1" :: "v"(o < last ? o : last), "s"(uni(sy.code)), "s"(uni(dst)) : "memory"); }
DI void flag_wait(const Sync& sy, int ph, int idx, int n, int lane) {
  const GAS u64* f = (const GAS u64*)(sy.ws + WS_FLAG + (ph * 256 + idx + (lane < n ? lane : n - 1)) * 8);
  const u64 want = tag_of(sy, ph);
  for (int spin = 0; spin < 40000; ++spin) {
    const u64 v = __hip_atomic_load(f, __ATOMIC_RELAXED, __HIP_MEMORY_SCOPE_AGENT);
    if (__all(v == want)) break;
    __builtin_amdgcn_s_sleep(2);
  }
  asm volatile("" ::: "memory");
}

DI void flag_wait_relay(const Sync& sy, int ph, int idx, int n, int lane, bool lead, lchar* word) {
  if (lead) { flag_wait(sy, ph, idx, n, lane); if (lane == 0) *(volatile LAS unsigned*)word = (unsigned)ph + 1u; }
  else { for (int spin = 0; spin < 4000000; ++spin) { if (__builtin_amdgcn_readfirstlane(*(volatile LAS unsigned*)word) == (unsigned)ph + 1u) break; __builtin_amdgcn_s_sleep(1); } }
  asm volatile("" ::: "memory");
}

constexpr int QSLOT = 34816, QA = 0, QW = 16384;
constexpr int T_SQ = 162048, T_SX = T_SQ + 640, T_CS = T_SX + 640;
constexpr int IMG = 104448;
constexpr int IA_Q = IMG, IA_KV = IMG + 12288, I_DUMMY = IMG + 49152;
constexpr int TOUCH0 = 150016;

DI void proj_w_issue(lchar* lds, const char* pw, int ct, int w, int lane, int g0, int g1) {
  const unsigned lds0 = (unsigned)(uintptr_t)lds;
  for (int g = g0 + w; g < g1; g += 8) dma4(lane * 16, pw + (size_t)ct * 102400 + g * 4096, lds0 + g * 4096);
}
DI void qkv_prefetch(lchar* lds, const char* PW, int head, int part) {
  const int lane = threadIdx.x & 63, w = __builtin_amdgcn_readfirstlane(threadIdx.x >> 6);
  const unsigned lds0 = (unsigned)(uintptr_t)lds;
  if (part == 0) {
#pragma unroll
    for (int i = 0; i < 3; ++i) { const int k0 = w + 8 * i, k = k0 < 19 ? k0 : 18; dma2(lane * 16, PW + (size_t)head * 81920 + k * 2048, lds0 + 102400 + k * 2048); } }
  else if (w >= 4) { for (int k = w - 4; k < 21; k += 4) dma2(lane * 16, PW + (size_t)head * 81920 + (19 + k) * 2048, lds0 + k * 2048); }
}

DI void phase_first(lchar* lds, const Params& p, const Sync& sy, int bx, int bj) {
  const int tid = threadIdx.x, lane = tid & 63, w = __builtin_amdgcn_readfirstlane(tid >> 6);
  const unsigned lds0 = (unsigned)(uintptr_t)lds;
  const int rt = 2 * bx + (bj & 1), hp = bj >> 1;
  const int h = lane >> 5, l31 = lane & 31;
  const char* W = p.wqA;
  rsrc_t rs = __builtin_amdgcn_make_buffer_rsrc((void*)p.ws, 0, WS_END, 0x00020000);
  f32x16 acc[2][2];
  int eo[2][4];
  if (w >= 4) {
    const int lw = w - 4;
    unsigned wv[4], av[4]; u64 wmk[4];
#pragma unroll
    for (int i = 0; i < 4; ++i) { const int q = lane >> 4, c = (lane & 15) ^ ((q << 2) | i), cc = c == 15 ? 14 : c;
      wv[i] = (q + 4 * i) * 3840 + (cc / 5) * 1280 + 80 * hp + (cc % 5) * 16 - 1024 * i; wmk[i] = __ballot(c != 15);
      av[i] = ((lane >> 3) + 8 * i) * 1280 + (((lane & 7) ^ ((4 * i + (lane >> 4)) & 7)) * 16) - 1024 * i; }
    auto issue = [&](int t) {
      dma4vm(wv[0], wv[1], wv[2], wv[3], W + (size_t)(64 * t + 16 * lw) * 3840, lds0 + (t & 3) * QSLOT + QW + lw * 4096, wmk[0], wmk[1], wmk[2], wmk[3]);
      dma4v(av[0], av[1], av[2], av[3], p.x0 + (size_t)(128 * rt + 32 * lw) * 1280 + t * 128, lds0 + (t & 3) * QSLOT + QA + lw * 4096);
    };
#pragma unroll
    for (int t = 0; t < 4; ++t) issue(t);
    const int g = lane >> 4, q4 = (lane & 15) >> 2, p4 = lane & 3;
    const lchar* hw[2][2];
#pragma unroll
    for (int n = 0; n < 2; ++n)
#pragma unroll
      for (int tt = 0; tt < 2; ++tt) { const int row = 8 * g + 4 * tt + q4, ch = 2 * (2 * lw + n) + (p4 >> 1);
        hw[n][tt] = lds + QW + row * 256 + (ch ^ (((row & 3) << 2) | ((row >> 2) & 3))) * 16 + 8 * (p4 & 1); }
    const lchar* sx_[2];
#pragma unroll
    for (int s = 0; s < 2; ++s) sx_[s] = lds + QA + (32 * lw + (lane & 15)) * 128 + (((4 * s + g) ^ (((lane & 15) >> 1) & 7)) * 16);
    f32x4 xx[2] = {{0.f, 0.f, 0.f, 0.f}, {0.f, 0.f, 0.f, 0.f}}, hacc[2] = {{0.f, 0.f, 0.f, 0.f}, {0.f, 0.f, 0.f, 0.f}};
    const bf16x8 ones8 = {0x3f80, 0x3f80, 0x3f80, 0x3f80, 0x3f80, 0x3f80, 0x3f80, 0x3f80};
#define HELP(T) do { const int so = ((T) & 3) * QSLOT; bf16x8 hwf[2][2]; \
      _Pragma("unroll") for (int s = 0; s < 2; ++s) _Pragma("unroll") for (int n = 0; n < 2; ++n) hwf[s][n] = cat(lds_tr(hw[n][0] + so + s * 8192), lds_tr(hw[n][1] + so + s * 8192)); \
      _Pragma("unroll") for (int s = 0; s < 2; ++s) _Pragma("unroll") for (int n = 0; n < 2; ++n) hacc[n] = MFMA16(hwf[s][n], ones8, hacc[n]); \
      _Pragma("unroll") for (int s = 0; s < 2; ++s) _Pragma("unroll") for (int m = 0; m < 2; ++m) { const bf16x8 f_ = lds_b128(sx_[s] + so + m * 2048); xx[m] = MFMA16(f_, f_, xx[m]); } } while (0)
#define LSTAGE(T, N) do { wait_vm<N>(); bar(); if ((T) <= 5) { bar_lgkm(); issue((T) + 4); } HELP((T) + 1); } while (0)
    wait_vm<24>(); bar_lgkm(); HELP(0);
    LSTAGE(0, 16); LSTAGE(1, 16); LSTAGE(2, 16); LSTAGE(3, 16); LSTAGE(4, 16); LSTAGE(5, 16); LSTAGE(6, 16); LSTAGE(7, 8); LSTAGE(8, 0);
#undef HELP
#undef LSTAGE
    const int c = lane & 15;
#pragma unroll
    for (int m = 0; m < 2; ++m) { const float d = (c & 2) ? ((c & 1) ? xx[m][3] : xx[m][2]) : ((c & 1) ? xx[m][1] : xx[m][0]);
      if (g == (c >> 2)) *(LAS float*)(lds + T_SQ + (16 + 32 * lw + 16 * m + c) * 4) = d; }
    if (c == 0) { *(LAS f32x4*)(lds + T_CS + (4 * (2 * lw) + g) * 16) = hacc[0]; *(LAS f32x4*)(lds + T_CS + (4 * (2 * lw + 1) + g) * 16) = hacc[1]; }
  } else {
    const int wm = w >> 1, wn = w & 1;
    {
      const int L = blockIdx.x * 155 + w * 39 + (lane < 39 ? lane : 38), Lc = L < 39680 ? L : 39679;
      const char* a_ = Lc < 19200 ? W + (size_t)Lc * 128 : p.x0 + (size_t)(Lc - 19200) * 128;
      asm volatile("s_mov_b32 m0, %1\n\ts_nop 3\n\tglobal_load_lds_dword %0, off" :: "v"(a_), "s"(uni(lds0 + TOUCH0 + 256 + w * 256)) : "memory"); }
    {
      constexpr int NR = (WS_END >> 21) + 1;
      static_assert(NR <= 32, "one page per workgroup of the group");
      if (tid == 192 && bj < NR) { const char* a_ = p.ws + ((size_t)bj << 21) + (bj == 0 ? 28672 : 0);
        asm volatile("global_atomic_or %0, %1, off sc1" :: "v"(a_), "v"(0u) : "memory"); } }
    { const int pc = 15 ^ (((lane & 3) << 2) | ((lane >> 2) & 3));
      *(LAS u32x4*)(lds + w * QSLOT + QW + lane * 256 + pc * 16) = (u32x4){0x3f803f80u, 0x3f803f80u, 0x3f803f80u, 0x3f803f80u}; }
    const int gw = blockIdx.x * 4 + w;
    u32x4 pk[7];
#pragma unroll
    for (int k = 0; k < 5; ++k) {
      const int u = gw + 1024 * k, m = u >= 2560, r = u - 2560 * m, hd = r / 80, row = 8 * (r % 80) + (lane >> 3), lc = (lane & 7) ^ (((lane >> 4) & 1) << 2);
      const char* rowp = (m ? p.wqB : p.wqA) + (size_t)row * 3840 + hd * 40;
      const int qa = 2 * lc, qb = lc == 7 ? 14 : 2 * lc + 1, ma = qa / 5, mb = qb / 5, pa = qa - 5 * ma, pb = qb - 5 * mb;
      const int da = (hd & 1) ? (pa == 4 ? 0 : pa + 1) : pa, db = (hd & 1) ? (pb == 4 ? 0 : pb + 1) : pb;
      const u32x2 lo = *(const u32x2*)(rowp + ma * 1280 + da * 8), hi = *(const u32x2*)(rowp + mb * 1280 + db * 8);
      pk[k] = (u32x4){lo[0], lo[1], hi[0], hi[1]};
    }
#pragma unroll
    for (int k = 5; k < 7; ++k) {
      const int u = gw + 1024 * k, v = (u < 6720 ? u : 5120) - 5120, m = v >= 800, r = v - 800 * m, ct = r / 100, idx = 64 * (r % 100) + lane, pr = idx / 10, ch = idx - 10 * pr;
      const int row = (pr & ~12) | ((pr & 4) << 1) | ((pr & 8) >> 1);
      pk[k] = *(const u32x4*)((m ? p.woB : p.woA) + (size_t)row * 1280 + ct * 160 + ch * 16);
    }
    const lchar* xa[2][4];
#pragma unroll
    for (int a = 0; a < 2; ++a) { const int mt = a ? 1 - wn : wn;
#pragma unroll
      for (int ks = 0; ks < 4; ++ks) xa[a][ks] = lds + QA + (64 * wm + 32 * mt + l31) * 128 + (((2 * ks + h) ^ ((l31 >> 1) & 7)) * 16); }
    const int blk = (lane >> 4) & 1, q4 = (lane & 15) >> 2, p4 = lane & 3;
    const lchar* wa[2][2];
#pragma unroll
    for (int b = 0; b < 2; ++b) { const int nt = b ? 1 - wm : wm;
#pragma unroll
      for (int tt = 0; tt < 2; ++tt) { const int row = 8 * h + 4 * tt + q4, ch = 4 * (2 * wn + nt) + 2 * blk + (p4 >> 1);
        wa[b][tt] = lds + QW + row * 256 + (ch ^ (((row & 3) << 2) | ((row >> 2) & 3))) * 16 + 8 * (p4 & 1); } }
#pragma unroll
    for (int a = 0; a < 2; ++a)
#pragma unroll
      for (int b = 0; b < 2; ++b) acc[a][b] = (f32x16){};
    bf16x8 xf[4][2], wf[4][2];
#define XRD(BUF, P, KS, a) xf[BUF][a] = lds_b128(P[a][KS])
#define WRD(BUF, P, KS, b) wf[BUF][b] = cat(lds_tr(P[b][0] + (KS) * 4096), lds_tr(P[b][1] + (KS) * 4096))
#define KSTEP(C, N, PX, PW, KS) do { \
      acc[0][0] = MFMA32(wf[C][0], xf[C][0], acc[0][0]); SBAR(); \
      XRD(N, PX, KS, 0); WRD(N, PW, KS, 0); SBAR(); \
      acc[0][1] = MFMA32(wf[C][1], xf[C][0], acc[0][1]); SBAR(); \
      WRD(N, PW, KS, 1); SBAR(); \
      acc[1][0] = MFMA32(wf[C][0], xf[C][1], acc[1][0]); SBAR(); \
      XRD(N, PX, KS, 1); SBAR(); \
      acc[1][1] = MFMA32(wf[C][1], xf[C][1], acc[1][1]); SBAR(); } while (0)
#pragma unroll
    for (int b = 0; b < 2; ++b)
#pragma unroll
      for (int g = 0; g < 4; ++g) {
        const int qi = 8 * (2 * wn + (b ? 1 - wm : wm)) + 2 * g + h;
        const int mat = (qi >= 10) + (qi >= 20), rem = qi - 10 * mat, hh = rem >= 5, qd = rem - 5 * hh;
        const int pos = hh ? (qd == 0 ? 4 : qd - 1) : qd;
        const int qk = (mat ? IA_KV + hh * 12288 : IA_Q + hh * 6144) + (pos >> 1) * 512 + l31 * 16 + (pos & 1) * 8;
        const int vv = IA_KV + hh * 12288 + KVV + pos * 256 + (((l31 >> 2) ^ pos) * 32) + (l31 & 3) * 8;
        eo[b][g] = qi >= 30 ? (I_DUMMY + lane * 8) | 4 : (mat == 2 ? vv : qk) | (mat == 0 ? 2 : 0);
        PIN(eo[b][g]);
      }
    bar_lgkm();
    XRD(0, xa, 0, 0); XRD(0, xa, 0, 1); WRD(0, wa, 0, 0); WRD(0, wa, 0, 1);
    XRD(1, xa, 1, 0); XRD(1, xa, 1, 1); WRD(1, wa, 1, 0); WRD(1, wa, 1, 1); SBAR();
#pragma unroll 1
    for (int T = 0; T < 10; ++T) {
      const int dn = (T & 3) == 3 ? -3 * QSLOT : QSLOT;
      const lchar* xn[2][4]; const lchar* wn_[2][2];
#pragma unroll
      for (int a = 0; a < 2; ++a) {
#pragma unroll
        for (int ks = 0; ks < 4; ++ks) xn[a][ks] = xa[a][ks] + dn;
        wn_[a][0] = wa[a][0] + dn; wn_[a][1] = wa[a][1] + dn; }
      KSTEP(0, 2, xa, wa, 2); KSTEP(1, 3, xa, wa, 3);
      if (T < 9) bar();
      KSTEP(2, 0, xn, wn_, 0); KSTEP(3, 1, xn, wn_, 1);
      if (T <= 5) bar();
#pragma unroll
      for (int a = 0; a < 2; ++a) {
#pragma unroll
        for (int ks = 0; ks < 4; ++ks) xa[a][ks] = xn[a][ks];
        wa[a][0] = wn_[a][0]; wa[a][1] = wn_[a][1]; }
    }
#undef XRD
#undef WRD
#undef KSTEP
    const bool onesq = ((lane & 7) ^ (((lane >> 4) & 1) << 2)) == 7;
#pragma unroll
    for (int k = 0; k < 5; ++k) { pk[k][2] = onesq ? 0x3f803f80u : pk[k][2]; pk[k][3] = onesq ? 0x3f803f80u : pk[k][3]; }
    asm volatile("" : "+v"(pk[0]), "+v"(pk[1]), "+v"(pk[2]), "+v"(pk[3]), "+v"(pk[4]), "+v"(pk[5]), "+v"(pk[6]));
#pragma unroll
    for (int k = 0; k < 7; ++k) { const int u = gw + 1024 * k;
      const unsigned dst = k < 5 ? WS_PW + u * 1024 : (u < 6720 ? WS_PO + (u - 5120) * 1024 : WS_END);
      __builtin_amdgcn_raw_buffer_store_b128(pk[k], rs, dst + lane * 16, 0, SC1); }
    if (lane < 32 && wn == 1) {
      *(LAS float*)(lds + T_SX + (16 + 64 * wm + 32 + lane) * 4) = wm ? acc[0][0][12] : acc[0][1][12];
      *(LAS float*)(lds + T_SX + (16 + 64 * wm + lane) * 4) = wm ? acc[1][0][12] : acc[1][1][12];
    }
  }
  bar_lgkm();
  if (w < 4) {
    const int wm = w >> 1, wn = w & 1;
    f32x4 cs[2][4]; float mu[2], rstd[2];
#pragma unroll
    for (int b = 0; b < 2; ++b)
#pragma unroll
      for (int g = 0; g < 4; ++g) cs[b][g] = *(LAS f32x4*)(lds + T_CS + (8 * (2 * wn + (b ? 1 - wm : wm)) + 2 * g + h) * 16);
#pragma unroll
    for (int a = 0; a < 2; ++a) { const int tt = 16 + 32 * (2 * wm + (a ? 1 - wn : wn)) + l31;
      ln_scalars(*(LAS float*)(lds + T_SX + tt * 4), *(LAS float*)(lds + T_SQ + tt * 4), mu[a], rstd[a]); }
    SBAR();
#pragma unroll
    for (int b = 0; b < 2; ++b)
#pragma unroll
      for (int g = 0; g < 4; ++g) {
        const int e = eo[b][g], base = e & ~7;
        const float sc = (e & 2) ? QSCALE : 1.0f;
        const int tstr = (e & 4) ? 0 : 1536;
#pragma unroll
        for (int a = 0; a < 2; ++a) {
          const int tl = 2 * wm + (a ? 1 - wn : wn);
          const float r = rstd[a] * sc, rm = -r * mu[a];
          const u32x2 o2 = {pkbf(__builtin_fmaf(r, acc[a][b][4 * g], rm * cs[b][g][0]), __builtin_fmaf(r, acc[a][b][4 * g + 1], rm * cs[b][g][1])),
                            pkbf(__builtin_fmaf(r, acc[a][b][4 * g + 2], rm * cs[b][g][2]), __builtin_fmaf(r, acc[a][b][4 * g + 3], rm * cs[b][g][3]))};
          *(LAS u32x2*)(lds + base + tl * tstr) = o2;
        }
      }
  } else {
    const int lw = w - 4, hh = lw >> 1;
#pragma unroll
    for (int i = 0; i < 2; ++i) { const int tl = 2 * (lw & 1) + i;
      if (h == 0) *(LAS u32x2*)(lds + IA_KV + hh * 12288 + tl * 1536 + 1024 + l31 * 16 + 8) = (u32x2){0x3f80u, 0u};
      else *(LAS u32x2*)(lds + IA_KV + hh * 12288 + KVV + tl * 1536 + 5 * 256 + l31 * 8) = (u32x2){0x3f80u, 0u}; }
  }
  bar_lgkm();
  u32x4 cv[5];
#pragma unroll
  for (int i = 0; i < 5; ++i) { const int u = w + 8 * i; cv[i] = *(LAS u32x4*)(lds + IMG + (u < 36 ? u : 0) * 1024 + lane * 16); }
#pragma unroll
  for (int i = 0; i < 5; ++i) { const int u = w + 8 * i;
    if (i < 4 || w < 4) {
      unsigned dst;
      if (u < 12) { const int hh = u >= 6; dst = WS_Q + ((2 * hp + hh) * 64 + 4 * rt) * 1536 + (u - 6 * hh) * 1024; }
      else { const int hh = u >= 24; dst = WS_KV + ((2 * hp + hh) * 16 + rt) * KVSTAGE + (u - 12 - 12 * hh) * 1024; }
      __builtin_amdgcn_raw_buffer_store_b128(cv[i], rs, dst + lane * 16, 0, SC1);
    } }
  bar_vm_lgkm();
  if (tid == 0) flag_set(sy, 0, hp * 16 + rt);
}

constexpr int RS0 = 139264, RS1 = 114688, RS2 = 90112;
constexpr int W1 = 102400, WKS1 = 19;
constexpr int U_HX = 43008;
constexpr int U_CS = 81920, U_SXH = U_CS + 256, U_DUMMY = U_CS + 512, U_FLG = U_CS + 1024;
DI int wks_off(int ks) { return ks < WKS1 ? W1 + ks * 2048 : (ks - WKS1) * 2048; }
constexpr int UA_Q = RS1;
constexpr int UB_Q = 90112, UB_K = UB_Q + 13824, UB_V = UB_K + 13824;
template <bool BL, int PH>
DI void phase_qkv2(lchar* lds, const Params& p, const Sync& sy, unsigned xoff, unsigned sqoff, unsigned kvoff, unsigned ooff, const char* nextw, const char* touchw, int x, int head) {
  const int tid = threadIdx.x, lane = tid & 63, w = __builtin_amdgcn_readfirstlane(tid >> 6);
  const int h = lane >> 5, l31 = lane & 31;
  rsrc_t rs = __builtin_amdgcn_make_buffer_rsrc((void*)p.ws, 0, WS_END, 0x00020000);
  f32x16 acc[2][2];
  f32x4 hacc = {0.f, 0.f, 0.f, 0.f};
  float sqv[2] = {0.f, 0.f};
  int eo[2][4];
  if (w >= 4) {
    const int lw = w - 4, c = lane & 15, g = lane >> 4, q4 = c >> 2, p4 = lane & 3;
    const lchar* hw0 = lds + (8 * (g & 1) + q4) * 128 + (((lw >> 1) ^ ((q4 >> 1) & 1)) * 64) + 32 * (lw & 1) + 8 * p4;
    const int gk = g >> 1;
    const bf16x8 ones8 = {0x3f80, 0x3f80, 0x3f80, 0x3f80, 0x3f80, 0x3f80, 0x3f80, 0x3f80};
    f32x4 hf = {0.f, 0.f, 0.f, 0.f};
    if (BL) {
      const int htok = x == 0 ? 16 : 256 * x - 16;
      flag_wait(sy, PH - 1, x > 0 ? 32 * x - 8 : 0, 8, lane);

      const char* hb = p.ws + xoff + (size_t)(htok >> 5) * 40960 + lw * 5 * 2048;
      const unsigned hv = g * 512 + 256 + c * 16, hd = (unsigned)(uintptr_t)lds + U_HX + lw * 5 * 1024;
#pragma unroll
      for (int i = 0; i < 5; ++i) dma1(hv, hb + i * 2048, hd + i * 1024);
      asm volatile("s_waitcnt vmcnt(5)\n\ts_barrier" ::: "memory");
#pragma unroll
      for (int i = 0; i < 4; ++i) hf[i] = __uint_as_float(__builtin_amdgcn_raw_buffer_load_b32(rs, sqoff + (htok >> 5) * 2048 + (4 * g + i) * 128 + (16 + c) * 4, 0, SC1));
      asm volatile("s_waitcnt vmcnt(0)" : "+v"(hf) :: "memory");
      if (lane == 0) *(volatile LAS unsigned*)(lds + U_FLG + lw * 4) = 1u;
      for (int spin = 0; spin < 1000000; ++spin) { const unsigned f = *(volatile LAS unsigned*)(lds + U_FLG + (lane & 3) * 4); if (__all(f == 1u)) break; __builtin_amdgcn_s_sleep(1); }
      asm volatile("" ::: "memory");
      if (touchw) { l2_touch(touchw + (size_t)head * 81920, (unsigned)(uintptr_t)lds + 65536, lw, lane); if (lw == 0) l2_touch(touchw + (size_t)head * 81920, (unsigned)(uintptr_t)lds + 65536, 4, lane); }
    } else asm volatile("s_waitcnt vmcnt(0)\n\ts_barrier" ::: "memory");
    __builtin_amdgcn_s_setprio(2);
    f32x4 hacc2 = {0.f, 0.f, 0.f, 0.f};
#pragma unroll
    for (int b5 = 0; b5 < 4; ++b5) {
      bf16x8 wfr[5], hxf[5];
#pragma unroll
      for (int i = 0; i < 5; ++i) { const int s = 5 * b5 + i;
        if (BL) hxf[i] = lds_b128(lds + U_HX + (4 * s + g) * 256 + c * 16);
        const int o0_ = wks_off(2 * s), o1_ = wks_off(2 * s + 1);
        const lchar* a_ = hw0 + (gk ? o1_ : o0_);
        wfr[i] = cat(lds_tr(a_), lds_tr(a_ + 512)); }
      SBAR();
#pragma unroll
      for (int i = 0; i < 5; ++i) { const int s = 5 * b5 + i;
        const bf16x8 xf = BL ? (c == 0 ? ones8 : hxf[i]) : ones8;
        if (i & 1) hacc2 = MFMA16(wfr[i], xf, hacc2); else hacc = MFMA16(wfr[i], xf, hacc); }
      SBAR();
    }
    __builtin_amdgcn_s_setprio(0);
#pragma unroll
    for (int i = 0; i < 4; ++i) hacc[i] += hacc2[i];
    if (BL) {
      sqv[0] = swap32_sum(swap16_sum((hf[0] + hf[1]) + (hf[2] + hf[3])));
      if (lw == 3 && g == 3) *(LAS float*)(lds + U_SXH + c * 4) = hacc[0];
    }
    if (c == 0) *(LAS f32x4*)(lds + U_CS + (4 * lw + g) * 16) = hacc;
    if (BL) {
      const int o = ((head & 3) * 200 + lw * 50 + (lane < 50 ? lane : 49)) * 128;
      asm volatile("s_mov_b32 m0, %2\n\ts_nop 3\n\tglobal_load_lds_dword %0, %1 sc1" :: "v"(o), "s"(uni(nextw + (size_t)(head >> 2) * 102400)), "s"(uni((unsigned)(uintptr_t)lds + 66560)) : "memory"); }

  } else {
#pragma unroll
    for (int b = 0; b < 2; ++b)
#pragma unroll
      for (int g = 0; g < 4; ++g) {
        const int qi = 8 * b + 2 * g + h, mat = (qi >= 5) + (qi >= 10), pos = qi - 5 * mat;
        int off;
        if (!BL) {
          const int qo = UA_Q + 2 * w * 1536 + (pos >> 1) * 512 + l31 * 16 + (pos & 1) * 8;
          const int kb = RS0 + (w >> 1) * KVSTAGE + 2 * (w & 1) * 1536;
          const int ko = kb + KVK + (pos >> 1) * 512 + l31 * 16 + (pos & 1) * 8;
          const int vo = kb + KVV + pos * 256 + (((l31 >> 2) ^ pos) * 32) + (l31 & 3) * 8;
          off = mat == 0 ? qo : (mat == 1 ? ko : vo);
        } else {
          const int par = l31 & 1, idx = 8 + 32 * w + (l31 >> 1);
          const int qk = (mat ? UB_K : UB_Q) + par * 6912 + (pos >> 1) * 2304 + idx * 16 + (pos & 1) * 8;
          const int vo = UB_V + par * 7296 + pos * 1216 + idx * 8;
          off = mat == 2 ? vo : qk;
        }
        eo[b][g] = qi == 15 ? (U_DUMMY + lane * 8) | 4 : off | (BL && mat == 2 ? 1 : 0) | (mat == 0 ? 2 : 0);
        PIN(eo[b][g]);
      }
    flag_wait_relay(sy, PH - 1, 32 * x, 32, lane, w == 0, lds + RELAY);

    const unsigned xv0 = h * 512 + l31 * 16, xv1 = xv0 + 40960, xs = xoff + (8 * x + 2 * w) * 40960;
    constexpr int DEP = 8;
    u32x4 xr[DEP][2];
#pragma unroll
    for (int i = 0; i < DEP; ++i) { xr[i][0] = __builtin_amdgcn_raw_buffer_load_b128(rs, xv0, xs + i * 1024, SC1); xr[i][1] = __builtin_amdgcn_raw_buffer_load_b128(rs, xv1, xs + i * 1024, SC1); }
    SBAR();
    unsigned sqp[2][8];
#pragma unroll
    for (int a = 0; a < 2; ++a)
#pragma unroll
      for (int i = 0; i < 8; ++i) sqp[a][i] = __builtin_amdgcn_raw_buffer_load_b32(rs, sqoff + (8 * x + 2 * w + a) * 2048 + (8 * h + i) * 128 + l31 * 4, 0, SC1);
    SBAR();
    const int blk = (lane >> 4) & 1, q4 = (lane & 15) >> 2, p4 = lane & 3;
    const lchar* wa[2]; const lchar* wb_[2];
#pragma unroll
    for (int nt = 0; nt < 2; ++nt) { wa[nt] = lds + (8 * h + q4) * 128 + ((nt ^ ((q4 >> 1) & 1)) * 64) + 32 * blk + 8 * p4; wb_[nt] = wa[nt] + W1; PIN(wb_[nt]); }
#pragma unroll
    for (int a = 0; a < 2; ++a)
#pragma unroll
      for (int b = 0; b < 2; ++b) acc[a][b] = (f32x16){};
    bf16x8 wf[3][2];
#define WPTR(KS, nt) ((KS) < WKS1 ? wb_[nt] + (KS) * 2048 : wa[nt] + ((KS) - WKS1) * 2048)
#define WRD2(BUF, KS, nt) wf[BUF][nt] = cat(lds_tr(WPTR(KS, nt)), lds_tr(WPTR(KS, nt) + 512))
    WRD2(0, 0, 0); WRD2(0, 0, 1); WRD2(1, 1, 0); WRD2(1, 1, 1); SBAR();

#pragma unroll
    for (int ks = 0; ks < 40; ++ks) {
      if (ks == 12) bar();
      const bf16x8 x0 = __builtin_bit_cast(bf16x8, xr[ks % DEP][0]), x1 = __builtin_bit_cast(bf16x8, xr[ks % DEP][1]);
      acc[0][0] = MFMA32(wf[ks % 3][0], x0, acc[0][0]); SBAR();
      if (ks + 2 < 40) { WRD2((ks + 2) % 3, ks + 2, 0); } SBAR();
      acc[0][1] = MFMA32(wf[ks % 3][1], x0, acc[0][1]); SBAR();
      if (ks + DEP < 40) xr[ks % DEP][0] = __builtin_amdgcn_raw_buffer_load_b128(rs, xv0, xs + (ks + DEP) * 1024, SC1);
      SBAR();
      acc[1][0] = MFMA32(wf[ks % 3][0], x1, acc[1][0]); SBAR();
      if (ks + 2 < 40) { WRD2((ks + 2) % 3, ks + 2, 1); } SBAR();
      acc[1][1] = MFMA32(wf[ks % 3][1], x1, acc[1][1]); SBAR();
      if (ks + DEP < 40) xr[ks % DEP][1] = __builtin_amdgcn_raw_buffer_load_b128(rs, xv1, xs + (ks + DEP) * 1024, SC1);
      SBAR();
    }

#pragma unroll
    for (int a = 0; a < 2; ++a) { float t = 0.f;
#pragma unroll
      for (int i = 0; i < 8; ++i) t += __uint_as_float(sqp[a][i]);
      sqv[a] = swap32_sum(t); }
  }
  bar_lgkm();

  if (w < 4) {
    f32x4 cs[2][4]; float mu[2], rstd[2];
#pragma unroll
    for (int b = 0; b < 2; ++b)
#pragma unroll
      for (int g = 0; g < 4; ++g) cs[b][g] = *(LAS f32x4*)(lds + U_CS + (8 * b + 2 * g + h) * 16);
#pragma unroll
    for (int a = 0; a < 2; ++a) {
      auto r = __builtin_amdgcn_permlane32_swap(__float_as_uint(acc[a][1][12]), __float_as_uint(acc[a][1][12]), false, false);
      ln_scalars(__uint_as_float(r[1]), sqv[a], mu[a], rstd[a]); }
    SBAR();
#pragma unroll
    for (int b = 0; b < 2; ++b)
#pragma unroll
      for (int g = 0; g < 4; ++g) {
        const int e = eo[b][g], off = e & ~7;
        const float sc = (e & 2) ? QSCALE : 1.0f;
        const int tstr = (e & 4) ? 0 : (BL ? ((e & 1) ? 128 : 256) : 1536);
#pragma unroll
        for (int a = 0; a < 2; ++a) {
          const float r = rstd[a] * sc, rm = -r * mu[a];
          const u32x2 o2 = {pkbf(__builtin_fmaf(r, acc[a][b][4 * g], rm * cs[b][g][0]), __builtin_fmaf(r, acc[a][b][4 * g + 1], rm * cs[b][g][1])),
                            pkbf(__builtin_fmaf(r, acc[a][b][4 * g + 2], rm * cs[b][g][2]), __builtin_fmaf(r, acc[a][b][4 * g + 3], rm * cs[b][g][3]))};
          *(LAS u32x2*)(lds + off + a * tstr) = o2;
        }
      }
  } else if (BL) {
    const int lw = w - 4, g = lane >> 4, tt = lane & 15;
    float mu, rstd; ln_scalars(*(LAS float*)(lds + U_SXH + tt * 4), sqv[0], mu, rstd);
    const int qi = 4 * lw + g, mat = (qi >= 5) + (qi >= 10), pos = qi - 5 * mat, par = tt & 1, idx = tt >> 1;
    const f32x4 cs = *(LAS f32x4*)(lds + U_CS + qi * 16);
    int off = mat == 2 ? UB_V + par * 7296 + pos * 1216 + idx * 8 : UB_K + par * 6912 + (pos >> 1) * 2304 + idx * 16 + (pos & 1) * 8;
    if (qi < 5 || qi == 15) off = U_DUMMY + lane * 8;
    *(LAS u32x2*)(lds + off) = (u32x2){pkbf(rstd * (hacc[0] - mu * cs[0]), rstd * (hacc[1] - mu * cs[1])), pkbf(rstd * (hacc[2] - mu * cs[2]), rstd * (hacc[3] - mu * cs[3]))};
    const int pr = lw & 1;
    for (int i = lane; i < 144; i += 64) *(LAS u32x2*)(lds + (lw < 2 ? UB_Q : UB_K) + pr * 6912 + 4608 + i * 16 + 8) = (u32x2){0u, 0u};
    if (lw < 2) for (int i = lane; i < 152; i += 64) *(LAS u32x2*)(lds + UB_V + pr * 7296 + 5 * 1216 + i * 8) = (u32x2){0x3f80u, 0u};
  } else {
    const int lw = w - 4;
#pragma unroll
    for (int i = 0; i < 2; ++i) { const int tl = 2 * (lw & 1) + i, st = lw >> 1;
      if (h == 0) *(LAS u32x2*)(lds + RS0 + st * KVSTAGE + KVK + tl * 1536 + 1024 + l31 * 16 + 8) = (u32x2){0x3f80u, 0u};
      else *(LAS u32x2*)(lds + RS0 + st * KVSTAGE + KVV + tl * 1536 + 5 * 256 + l31 * 8) = (u32x2){0x3f80u, 0u}; }
  }
  bar_lgkm();

  if (!BL) {
    u32x4 cv[3];
#pragma unroll
    for (int i = 0; i < 3; ++i) cv[i] = *(LAS u32x4*)(lds + RS0 + (w * 3 + i) * 1024 + lane * 16);
#pragma unroll
    for (int i = 0; i < 3; ++i) __builtin_amdgcn_raw_buffer_store_b128(cv[i], rs, kvoff + (head * 16 + 2 * x) * KVSTAGE + (w * 3 + i) * 1024 + lane * 16, 0, SC1);

  } else {
    const int G = w, c = lane & 15, g = lane >> 4, q4 = c >> 2, p4 = lane & 3;
    const int iB = 8 + 16 * G, iA = iB - 16, ch = g < 3 ? g : 2, qd1 = p4 == 0 ? 4 : 5;
    const int ia = iA + c < 0 ? 0 : iA + c, ka = iA + 4 * g + q4 < 0 ? 0 : iA + 4 * g + q4, kb = iB + 4 * g + q4;
    bf16x8 qf[2], kA[2], kB[2], v0[2], v1[2];
#pragma unroll
    for (int par = 0; par < 2; ++par) {
      qf[par] = lds_b128(lds + UB_Q + par * 6912 + ch * 2304 + (iB + c) * 16);
      kA[par] = lds_b128(lds + UB_K + par * 6912 + ch * 2304 + ia * 16); kB[par] = lds_b128(lds + UB_K + par * 6912 + ch * 2304 + (iB + c) * 16);
      const lchar* vb = lds + UB_V + par * 7296;
      v0[par] = cat(lds_tr(vb + p4 * 1216 + ka * 8), lds_tr(vb + p4 * 1216 + kb * 8));
      v1[par] = cat(lds_tr(vb + qd1 * 1216 + ka * 8), lds_tr(vb + qd1 * 1216 + kb * 8));
    }
    f32x4 bA, bB;
#pragma unroll
    for (int i = 0; i < 4; ++i) { const int r = 4 * g + i;
      bB[i] = (r <= c && r >= c - 7) ? 0.f : -1e30f;
      bA[i] = (r >= c + 9 && !(x == 0 && G == 0)) ? 0.f : -1e30f; }
    SBAR();
    f32x4 sA[2], sB[2]; float mx[2];
#pragma unroll
    for (int par = 0; par < 2; ++par) { if (g == 3) qf[par] = (bf16x8){0, 0, 0, 0, 0, 0, 0, 0};
      sA[par] = MFMA16(kA[par], qf[par], bA); sB[par] = MFMA16(kB[par], qf[par], bB); }
#pragma unroll
    for (int par = 0; par < 2; ++par)
      mx[par] = __builtin_fmaxf(__builtin_fmaxf(__builtin_fmaxf(sA[par][0], sA[par][1]), __builtin_fmaxf(sA[par][2], sA[par][3])), __builtin_fmaxf(__builtin_fmaxf(sB[par][0], sB[par][1]), __builtin_fmaxf(sB[par][2], sB[par][3])));
#pragma unroll
    for (int par = 0; par < 2; ++par) mx[par] = swap16_max(mx[par]);
#pragma unroll
    for (int par = 0; par < 2; ++par) mx[par] = swap32_max(mx[par]);
    f32x4 t0[2], t1[2];
#pragma unroll
    for (int par = 0; par < 2; ++par) { const float m = mx[par];
      const u32x4 pw = {pkbf(ex2(sA[par][0] - m), ex2(sA[par][1] - m)), pkbf(ex2(sA[par][2] - m), ex2(sA[par][3] - m)), pkbf(ex2(sB[par][0] - m), ex2(sB[par][1] - m)), pkbf(ex2(sB[par][2] - m), ex2(sB[par][3] - m))};
      const bf16x8 pf = __builtin_bit_cast(bf16x8, pw); const f32x4 z = {0.f, 0.f, 0.f, 0.f};
      t0[par] = MFMA16(v0[par], pf, z); t1[par] = MFMA16(v1[par], pf, z); }
    u32x2 o0[2], o1[2];
#pragma unroll
    for (int par = 0; par < 2; ++par) {
      auto r1 = __builtin_amdgcn_permlane16_swap(__float_as_uint(t1[par][0]), __float_as_uint(t1[par][0]), false, false);
      auto r2 = __builtin_amdgcn_permlane32_swap(r1[1], r1[1], false, false);
      const float inv = __builtin_amdgcn_rcpf(__uint_as_float(r2[0]));
      o0[par] = (u32x2){pkbf(t0[par][0] * inv, t0[par][1] * inv), pkbf(t0[par][2] * inv, t0[par][3] * inv)};
      o1[par] = (u32x2){pkbf(t1[par][0] * inv, t1[par][1] * inv), pkbf(t1[par][2] * inv, t1[par][3] * inv)};
    }
    const int cA = (5 * head + 1) >> 1, odd = head & 1;
    const unsigned ob = ooff + (8 * x + G) * 40960 + (2 * c + (g & 1)) * 16;
    auto rx = __builtin_amdgcn_permlane16_swap(o0[0][0], o0[1][0], false, false), ry = __builtin_amdgcn_permlane16_swap(o0[0][1], o0[1][1], false, false);
    __builtin_amdgcn_raw_buffer_store_b128((u32x4){rx[0], ry[0], rx[1], ry[1]}, rs, ob + (cA + (g >> 1)) * 512, 0, SC1);
    auto sx2 = __builtin_amdgcn_permlane16_swap(o1[0][0], o1[1][0], false, false), sy2 = __builtin_amdgcn_permlane16_swap(o1[0][1], o1[1][1], false, false);
    if (g < 2) __builtin_amdgcn_raw_buffer_store_b64((u32x2){sx2[0], sy2[0]}, rs, ob + (odd ? cA - 1 : cA + 2) * 512 + odd * 8, 0, SC1);

    bar_vm_lgkm();
    if (tid == 0) flag_set(sy, PH, x * 32 + head);
    proj_w_issue(lds, nextw, head >> 2, w, lane, 0, 25);
  }
}

constexpr int VOTE = 106496;
DI int rslot(int s) { return RS0 - s * ROUND; }
template <int PH, bool FUSED>
DI void phase_attn(lchar* lds, const Params& p, const Sync& sy, unsigned qoff, unsigned kvoff, unsigned ooff, const char* nextw, const char* touchw, int x, int head) {
  const unsigned lds0 = (unsigned)(uintptr_t)lds;
  const int tid = threadIdx.x, lane = tid & 63, w = __builtin_amdgcn_readfirstlane(tid >> 6);
  const int h = lane >> 5, l31 = lane & 31;
  const int qg = w & 3, kh = w >> 2, hp = head >> 1;
  rsrc_t rs = __builtin_amdgcn_make_buffer_rsrc((void*)p.ws, 0, WS_END, 0x00020000);
  const char* kvbase = p.ws + kvoff + (size_t)head * 16 * KVSTAGE;
  const unsigned dvoff = lane * 16;
  auto issue = [&](int r, int slot) { dma3(dvoff, kvbase + w * 3072 + ((x + r) & 7) * ROUND, lds0 + rslot(slot) + w * 3072); };
  if (!FUSED) flag_wait(sy, PH - 1, hp * 16 + 2 * x, 2, lane);

  const int tb = 8 * x + 2 * qg;
  bf16x8 qA0, qB0; u32x2 qa1, qb1;
  if (FUSED) {
    qA0 = lds_b128(lds + UA_Q + 2 * qg * 1536 + h * 512 + l31 * 16); qB0 = lds_b128(lds + UA_Q + (2 * qg + 1) * 1536 + h * 512 + l31 * 16);
    qa1 = *(LAS u32x2*)(lds + UA_Q + 2 * qg * 1536 + 1024 + l31 * 16); qb1 = *(LAS u32x2*)(lds + UA_Q + (2 * qg + 1) * 1536 + 1024 + l31 * 16);
  } else {
    const unsigned qb = qoff + (head * 64 + tb) * 1536;
    qA0 = __builtin_bit_cast(bf16x8, __builtin_amdgcn_raw_buffer_load_b128(rs, qb + h * 512 + l31 * 16, 0, SC1));
    qB0 = __builtin_bit_cast(bf16x8, __builtin_amdgcn_raw_buffer_load_b128(rs, qb + 1536 + h * 512 + l31 * 16, 0, SC1));
    qa1 = __builtin_amdgcn_raw_buffer_load_b64(rs, qb + 1024 + l31 * 16, 0, SC1);
    qb1 = __builtin_amdgcn_raw_buffer_load_b64(rs, qb + 1536 + 1024 + l31 * 16, 0, SC1);
  }
  const int dg = (lane >> 4) & 1, qq = (lane & 15) >> 2, p4 = lane & 3;
  const int qd = dg ? (p4 == 0 ? 4 : 5) : p4;
  const f32x16 zero = {};
  s16x4 qA1, qB1;
  float o[12];
  for (int attempt = 0;; ++attempt) {
    const lchar* kp0 = lds + RS0 + kh * KVSTAGE + KVK + h * 512 + l31 * 16;
    const lchar* kp1 = lds + RS0 + kh * KVSTAGE + KVK + 1024 + l31 * 16 + h * 8;
    const lchar* vp00 = lds + RS0 + kh * KVSTAGE + KVV + qd * 256 + (((0 + h) ^ qd) * 32) + qq * 8;
    const lchar* vp01 = lds + RS0 + kh * KVSTAGE + KVV + qd * 256 + (((2 + h) ^ qd) * 32) + qq * 8;
    const lchar* vp10 = lds + RS0 + kh * KVSTAGE + KVV + qd * 256 + (((4 + h) ^ qd) * 32) + qq * 8;
    const lchar* vp11 = lds + RS0 + kh * KVSTAGE + KVV + qd * 256 + (((6 + h) ^ qd) * 32) + qq * 8;
    if (!FUSED || attempt) {
      issue(0, 0);
      PIN(kp0); PIN(kp1); PIN(vp00); PIN(vp01); PIN(vp10); PIN(vp11);
      asm volatile("s_waitcnt vmcnt(0)\n\ts_barrier" ::: "memory");
    }
    {
      const u32x2 ta = {h ? 0u : qa1[0], h ? 0u : qa1[1]}, tb2 = {h ? 0u : qb1[0], h ? 0u : qb1[1]};
      float ma, mb;
      if (attempt == 0) {
        const bf16x8 a0 = lds_b128(lds + RS0 + KVK + h * 512 + l31 * 16);
        const s16x4 a1 = lds_b64(lds + RS0 + KVK + 1024 + l31 * 16 + h * 8);
        f32x16 sa = MFMA32(a0, qA0, zero); sa = MFMA8(a1, __builtin_bit_cast(s16x4, ta), sa);
        f32x16 sb = MFMA32(a0, qB0, zero); sb = MFMA8(a1, __builtin_bit_cast(s16x4, tb2), sb);
        ma = sa[0]; mb = sb[0];
#pragma unroll
        for (int r = 1; r < 16; ++r) { ma = __builtin_fmaxf(ma, sa[r]); mb = __builtin_fmaxf(mb, sb[r]); }
      } else {
        ma = -INFINITY; mb = -INFINITY;
#pragma unroll 1
        for (int t = 0; t < 64; ++t) {
          const unsigned kb = kvoff + (head * 16 + (t >> 2)) * KVSTAGE + KVK + (t & 3) * 1536;
          const bf16x8 a0 = __builtin_bit_cast(bf16x8, __builtin_amdgcn_raw_buffer_load_b128(rs, kb + h * 512 + l31 * 16, 0, SC1));
          const s16x4 a1 = __builtin_bit_cast(s16x4, __builtin_amdgcn_raw_buffer_load_b64(rs, kb + 1024 + l31 * 16 + h * 8, 0, SC1));
          f32x16 sa = MFMA32(a0, qA0, zero); sa = MFMA8(a1, __builtin_bit_cast(s16x4, ta), sa);
          f32x16 sb = MFMA32(a0, qB0, zero); sb = MFMA8(a1, __builtin_bit_cast(s16x4, tb2), sb);
#pragma unroll
          for (int r = 0; r < 16; ++r) { ma = __builtin_fmaxf(ma, sa[r]); mb = __builtin_fmaxf(mb, sb[r]); }
        }
      }
      ma = swap32_max(ma); mb = swap32_max(mb);
      const unsigned na = pkbf(-ma, 0.f), nb = pkbf(-mb, 0.f);
      const u32x2 ua = {h ? na : qa1[0], h ? 0u : qa1[1]}, ub = {h ? nb : qb1[0], h ? 0u : qb1[1]};
      qA1 = __builtin_bit_cast(s16x4, ua); qB1 = __builtin_bit_cast(s16x4, ub);
    }
    f32x16 oA = {}, oB = {}, sA, sB;
    u32x4 pA0 = {}, pA1 = {}, pB0 = {}, pB1 = {};
    bf16x8 kf0, vf0, vf1; s16x4 kf1;
#define KRD(P0, P1, TL) do { kf0 = lds_b128((P0) + (TL) * 1536); kf1 = lds_b64((P1) + (TL) * 1536); } while (0)
#define VRD(TL) do { vf0 = cat(lds_tr(vp00 + (TL) * 1536), lds_tr(vp01 + (TL) * 1536)); vf1 = cat(lds_tr(vp10 + (TL) * 1536), lds_tr(vp11 + (TL) * 1536)); } while (0)
    KRD(kp0, kp1, 0); VRD(0);
    sA = MFMA32(kf0, qA0, zero); sA = MFMA8(kf1, qA1, sA);
#define SP(Y, S, g) do { if (Y) { if ((S) == 1 && (g) == 3) __builtin_amdgcn_s_setprio(0); if ((S) == 0 && (g) == 0) __builtin_amdgcn_s_setprio(1); } } while (0)
#define HSTEP(Y, S, SC, PC0, PC1, SN, QN0, QN1, ON, PN0, PN1, LOADK, LOADV) do { \
    SBAR(); SP(Y, S, 0); \
    SN = MFMA32(kf0, QN0, zero); SBAR(); \
    SC[0] = ex2(SC[0]); SC[1] = ex2(SC[1]); SC[2] = ex2(SC[2]); SC[3] = ex2(SC[3]); PIN(SC); SBAR(); SP(Y, S, 1); \
    SN = MFMA8(kf1, QN1, SN); SBAR(); \
    LOADK; \
    SC[4] = ex2(SC[4]); SC[5] = ex2(SC[5]); SC[6] = ex2(SC[6]); SC[7] = ex2(SC[7]); \
    PC0[0] = pkbf(SC[0], SC[1]); PC0[1] = pkbf(SC[2], SC[3]); PIN(SC); PIN(PC0); SBAR(); SP(Y, S, 2); \
    ON = MFMA32(vf0, __builtin_bit_cast(bf16x8, PN0), ON); SBAR(); \
    SC[8] = ex2(SC[8]); SC[9] = ex2(SC[9]); SC[10] = ex2(SC[10]); SC[11] = ex2(SC[11]); \
    PC0[2] = pkbf(SC[4], SC[5]); PC0[3] = pkbf(SC[6], SC[7]); PIN(SC); PIN(PC0); SBAR(); SP(Y, S, 3); \
    ON = MFMA32(vf1, __builtin_bit_cast(bf16x8, PN1), ON); SBAR(); \
    LOADV; \
    SC[12] = ex2(SC[12]); SC[13] = ex2(SC[13]); SC[14] = ex2(SC[14]); SC[15] = ex2(SC[15]); \
    PC1[0] = pkbf(SC[8], SC[9]); PC1[1] = pkbf(SC[10], SC[11]); \
    PC1[2] = pkbf(SC[12], SC[13]); PC1[3] = pkbf(SC[14], SC[15]); PIN(PC1); SBAR(); \
  } while (0)
#define NOP_ do {} while (0)
#define XSTEP(Y, LK, LV) HSTEP(Y, 0, sA, pA0, pA1, sB, qB0, qB1, oB, pB0, pB1, LK, LV)
#define YSTEP(Y) HSTEP(Y, 1, sB, pB0, pB1, sA, qA0, qA1, oA, pA0, pA1, NOP_, NOP_)
    int slot = 0;
    const char* wsl = nextw + (size_t)(head >> 2) * 102400;
    auto refill = [&](int r, int sl2) {
      if (r < 6) issue(r + 2, sl2);
      const int gi = r == 6 ? 21 + w : 8 * (r - 1) + w;
      if (((r >= 1 && r <= 3 && gi < 21) || (r == 6 && gi < 25))) dma4(dvoff, wsl + gi * 4096, lds0 + gi * 4096);
      const unsigned dd = (r < 6 ? lds0 + 86016 : lds0 + 102400) + w * 256;
      if (r >= 2 && r <= 6 && w < 2) l2_touch(touchw + (size_t)head * 81920, dd, 2 * (r - 2) + w, lane);
    };
#define ROUND_(Y, MID) do { \
      const int nslot = slot == 2 ? 0 : slot + 1, n2slot = slot == 0 ? 2 : slot - 1; \
      const int dn = rslot(nslot) - rslot(slot); \
      const lchar* kn0 = kp0 + dn; const lchar* kn1 = kp1 + dn; \
      XSTEP(Y, KRD(kp0, kp1, 1), VRD(0)); YSTEP(Y); \
      MID; \
      XSTEP(Y, KRD(kp0, kp1, 2), VRD(1)); YSTEP(Y); \
      XSTEP(Y, KRD(kp0, kp1, 3), VRD(2)); YSTEP(Y); \
      asm volatile("s_waitcnt vmcnt(0)\n\ts_barrier" ::: "memory"); \
      if (!Y) refill(r, n2slot); \
      XSTEP(Y, KRD(kn0, kn1, 0), VRD(3)); YSTEP(Y); \
      kp0 = kn0; kp1 = kn1; vp00 += dn; vp01 += dn; vp10 += dn; vp11 += dn; slot = nslot; } while (0)
#define MID0 do { \
        if (FUSED) { if (attempt == 0) {     \
            asm volatile("s_waitcnt vmcnt(0)\n\ts_barrier" ::: "memory"); \
            if (tid == 0) flag_set(sy, PH - 1, head * 8 + x); \
            if (w == 0) flag_wait(sy, PH - 1, head * 8, 8, lane); } \
        } else { \
          if (attempt == 0 && w == 0) flag_wait(sy, PH - 1, hp * 16, 16, lane); \
          if (attempt == 0 && PH == 1 && w == 1) { flag_wait(sy, 0, 0, 64, lane); flag_wait(sy, 0, 64, 64, lane); flag_wait(sy, 0, 128, 64, lane); flag_wait(sy, 0, 192, 64, lane); } } \
        bar(); issue(1, 1); } while (0)
#define ROUNDS(Y) \
    { const int r = 0; ROUND_(Y, MID0); } \
    for (int r = 1; r < 8; ++r) ROUND_(Y, if (Y) refill(r - 1, nslot));
    if (w >= 4) { ROUNDS(true) __builtin_amdgcn_s_setprio(0); } else { ROUNDS(false) }
    oB = MFMA32(vf0, __builtin_bit_cast(bf16x8, pB0), oB);
    oB = MFMA32(vf1, __builtin_bit_cast(bf16x8, pB1), oB);

    LAS f32x4* xb = (LAS f32x4*)(lds + RS0) + (qg * 2) * 192 + lane;
    if (kh) { xb[0] = (f32x4){oA[0], oA[1], oA[2], oA[3]}; xb[64] = (f32x4){oA[4], oA[5], oA[6], oA[7]}; xb[128] = (f32x4){oA[8], oA[9], oA[10], oA[11]}; }
    else { xb[192] = (f32x4){oB[0], oB[1], oB[2], oB[3]}; xb[192 + 64] = (f32x4){oB[4], oB[5], oB[6], oB[7]}; xb[192 + 128] = (f32x4){oB[8], oB[9], oB[10], oB[11]}; }
    if (tid == 0) *(LAS unsigned*)(lds + VOTE) = 0u;
    bar_lgkm();
    const int sel = kh ? 192 : 0;
    const f32x4 a = xb[sel], b = xb[sel + 64], c = xb[sel + 128];
#pragma unroll
    for (int i = 0; i < 4; ++i) { o[i] = (kh ? oB[i] : oA[i]) + a[i]; o[4 + i] = (kh ? oB[4 + i] : oA[4 + i]) + b[i]; o[8 + i] = (kh ? oB[8 + i] : oA[8 + i]) + c[i]; }
    auto rr = __builtin_amdgcn_permlane32_swap(__float_as_uint(o[8]), __float_as_uint(o[8]), false, false);
    const float l = __uint_as_float(rr[1]);
    const float inv = __builtin_amdgcn_rcpf(l);
#pragma unroll
    for (int i = 0; i < 12; ++i) o[i] *= inv;
    const int cA = (5 * head + 1) >> 1, odd = head & 1;
    const unsigned ob = ooff + (tb + kh) * 40960 + l31 * 16;
    {
      auto sx_ = __builtin_amdgcn_permlane32_swap(pkbf(o[0], o[1]), pkbf(o[4], o[5]), false, false), sy_ = __builtin_amdgcn_permlane32_swap(pkbf(o[2], o[3]), pkbf(o[6], o[7]), false, false);
      __builtin_amdgcn_raw_buffer_store_b128((u32x4){sx_[0], sy_[0], sx_[1], sy_[1]}, rs, ob + (cA + h) * 512, 0, SC1); }
    if (h == 0) __builtin_amdgcn_raw_buffer_store_b64((u32x2){pkbf(o[8], o[9]), pkbf(o[10], o[11])}, rs, ob + (odd ? cA - 1 : cA + 2) * 512 + odd * 8, 0, SC1);
    const bool bad = !(l >= 8.6736174e-19f && l <= 1.1529215e18f);
    if (attempt == 0 && __any(bad) && lane == 0) *(LAS unsigned*)(lds + VOTE) = 1u;
    bar_vm_lgkm();
    const unsigned redo = __builtin_amdgcn_readfirstlane(*(LAS unsigned*)(lds + VOTE));
    if (!redo || attempt == 1) break;
    bar_lgkm();
  }

  if (tid == 0) flag_set(sy, PH, x * 32 + head);
}

constexpr int P_RED = 141312;
template <bool FIRST, bool LAST, int PH>
DI void phase_proj(lchar* lds, const Params& p, const Sync& sy, unsigned ooff, unsigned xin, unsigned xout, unsigned sqout, const char* nextw, int bx, int bj) {
  const int tid = threadIdx.x, lane = tid & 63, w = __builtin_amdgcn_readfirstlane(tid >> 6);
  const int rt = 4 * bx + (bj & 3), ct = bj >> 2;
  const int wm = w & 3, wk = w >> 2, tok16 = lane & 15, g = lane >> 4;
  rsrc_t rs = __builtin_amdgcn_make_buffer_rsrc((void*)p.ws, 0, WS_END, 0x00020000);
  const int token = 64 * rt + 16 * wm + tok16;
  const unsigned xtok = (token >> 5) * 40960 + (token & 31) * 16;
  u32x2 res[3];
#pragma unroll
  for (int j = 0; j < 3; ++j) { const int t = wk ? 3 + (j & 1) : j; const int quad = 20 * ct + 4 * t + g;
    if (FIRST) res[j] = *(const u32x2*)(p.x0 + (size_t)token * 1280 + quad * 8);
    else res[j] = __builtin_amdgcn_raw_buffer_load_b64(rs, xin + xtok + (quad >> 1) * 512 + (quad & 1) * 8, 0, SC1); }
  if (tid == 0) *(volatile LAS unsigned*)(lds + RELAY) = 0u;
  bar_vm_lgkm();
  flag_wait_relay(sy, PH - 1, bx * 32, 32, lane, w == 0, lds + RELAY);

  u32x4 a[10];
  {
    const unsigned av = ooff + xtok + (40 * wk + g) * 512;
    const char* wsb = uni(p.ws);
#define ALD(s) asm volatile("global_load_dwordx4 %0, %1, %2 offset:0 sc1" : "=&v"(a[s]) : "v"(av + (s) * 2048), "s"(wsb) : "memory")
    ALD(0); ALD(1); ALD(2); ALD(3); ALD(4); ALD(5); ALD(6); ALD(7); ALD(8); ALD(9);
#undef ALD
  }

  f32x4 acc[5];
#pragma unroll
  for (int t = 0; t < 5; ++t) acc[t] = (f32x4){0.f, 0.f, 0.f, 0.f};
  const int q4 = (lane & 15) >> 2, p4 = lane & 3;
  const lchar* wb = lds + (320 * wk + 16 * (g >> 1) + 4 * (g & 1) + q4) * 160 + p4 * 8;
  bf16x8 pwf[2][5];
#define PWRD(s) do { _Pragma("unroll") for (int t = 0; t < 5; ++t) pwf[(s) & 1][t] = cat(lds_tr(wb + (s) * 5120 + t * 32), lds_tr(wb + (s) * 5120 + 1280 + t * 32)); } while (0)
#define PSTEP(s) do { if ((s) < 9) PWRD((s) + 1); SBAR(); asm volatile("s_waitcnt vmcnt(%1)" : "+v"(a[s]) : "i"(9 - (s) + ((LAST || (s) < 5) ? 0 : 6))); SBAR(); \
    const bf16x8 bf = __builtin_bit_cast(bf16x8, a[s]); \
    _Pragma("unroll") for (int t = 0; t < 5; ++t) acc[t] = MFMA16(pwf[(s) & 1][t], bf, acc[t]); SBAR(); } while (0)
  PWRD(0);
  PSTEP(0); PSTEP(1); PSTEP(2); PSTEP(3); PSTEP(4);
  if (!LAST) qkv_prefetch(lds, nextw, bj, 0);
  PSTEP(5); PSTEP(6); PSTEP(7); PSTEP(8); PSTEP(9);
#undef PWRD
#undef PSTEP

  LAS f32x4* red = (LAS f32x4*)(lds + P_RED) + wm * 320 + lane;
  if (wk) { red[0] = acc[0]; red[64] = acc[1]; red[128] = acc[2]; } else { red[192] = acc[3]; red[256] = acc[4]; }
  bar_lgkm();

  float sq = 0.f;
  u32x2 pk[3];
#pragma unroll
  for (int j = 0; j < 3; ++j) {
    const int t = wk ? 3 + (j & 1) : j;
    const f32x4 o = red[t * 64];
    const f32x4 me = wk ? ((j & 1) ? acc[4] : acc[3]) : acc[j];
    pk[j] = (u32x2){pkbf(me[0] + o[0] + bflo(res[j][0]), me[1] + o[1] + bfhi(res[j][0])), pkbf(me[2] + o[2] + bflo(res[j][1]), me[3] + o[3] + bfhi(res[j][1]))};
    if (j < 2 || !wk) { const float r0 = bflo(pk[j][0]), r1 = bfhi(pk[j][0]), r2 = bflo(pk[j][1]), r3 = bfhi(pk[j][1]);
      sq += (r0 * r0 + r1 * r1) + (r2 * r2 + r3 * r3); }
  }
  if (LAST) {
#pragma unroll
    for (int j = 0; j < 3; ++j) { if (wk && j == 2) break; *(u32x2*)(p.out + (size_t)token * 1280 + (20 * ct + 4 * (wk ? 3 + j : j) + g) * 8) = pk[j]; }
  } else {
    auto sx_ = __builtin_amdgcn_permlane16_swap(pk[0][0], pk[1][0], false, false), sy_ = __builtin_amdgcn_permlane16_swap(pk[0][1], pk[1][1], false, false);
    __builtin_amdgcn_raw_buffer_store_b128((u32x4){sx_[0], sy_[0], sx_[1], sy_[1]}, rs, xout + xtok + (10 * ct + 2 * ((wk ? 3 : 0) + (g & 1)) + (g >> 1)) * 512, 0, SC1);
    if (!wk) __builtin_amdgcn_raw_buffer_store_b64(pk[2], rs, xout + xtok + (10 * ct + 4 + (g >> 1)) * 512 + (g & 1) * 8, 0, SC1);
    sq = swap32_sum(swap16_sum(sq));
    if (g == 0) __builtin_amdgcn_raw_buffer_store_b32(__float_as_uint(sq), rs, sqout + (token >> 5) * 2048 + (2 * ct + wk) * 128 + (token & 31) * 4, 0, SC1);
  }

  if (!LAST) {
    if (tid == 0) *(volatile LAS unsigned*)(lds + RELAY) = 0u;
    if (tid >= 64 && tid < 68) *(volatile LAS unsigned*)(lds + U_FLG + (tid - 64) * 4) = 0u;
    bar_vm_lgkm();
    if (tid == 0) flag_set(sy, PH, rt * 8 + ct);
    qkv_prefetch(lds, nextw, bj, 1);
  }
}

__global__ void __launch_bounds__(512, 2) k_stack(Params p) {
  __shared__ __attribute__((aligned(16))) char smem[163840];
  lchar* lds = (lchar*)smem;
  const int bx = blockIdx.x & 7, bj = blockIdx.x >> 3;
  Sync sy; sy.ws = p.ws;
  sy.nonce = __hip_atomic_load((const GAS u64*)(p.ws + WS_NONCE), __ATOMIC_RELAXED, __HIP_MEMORY_SCOPE_AGENT);
  { u64 pc; int len;
    asm volatile("s_getpc_b64 %0\n.Lstack_here:\n\ts_mov_b32 %1, .Lstack_tail-.Lstack_here" : "=s"(pc), "=s"(len));
    sy.code = (const char*)pc; sy.clen = len; }
  if (threadIdx.x >= 448) code_touch(sy, (unsigned)(uintptr_t)lds + TOUCH0, bj, threadIdx.x & 63);
  const char* PWA = p.ws + WS_PW; const char* PWB_ = p.ws + WS_PW + PWB; const char* POA = p.ws + WS_PO; const char* POBp = p.ws + WS_PO + POB_;
  phase_first(lds, p, sy, bx, bj);
  phase_attn<1, false>(lds, p, sy, WS_Q, WS_KV, WS_O, POA, PWB_, bx, bj);
  phase_proj<true, false, 2>(lds, p, sy, WS_O, 0, WS_X, WS_SQ, PWB_, bx, bj);
  phase_qkv2<true, 3>(lds, p, sy, WS_X, WS_SQ, 0, WS_O + XB, POBp, PWA, bx, bj);
  phase_proj<false, false, 4>(lds, p, sy, WS_O + XB, WS_X, WS_X + XB, WS_SQ + SQB, PWA, bx, bj);
  phase_qkv2<false, 5>(lds, p, sy, WS_X + XB, WS_SQ + SQB, WS_KV + KVBYTES, 0, nullptr, nullptr, bx, bj);
  phase_attn<6, true>(lds, p, sy, 0, WS_KV + KVBYTES, WS_O + 2 * XB, POA, PWB_, bx, bj);
  phase_proj<false, false, 7>(lds, p, sy, WS_O + 2 * XB, WS_X + XB, WS_X + 2 * XB, WS_SQ + 2 * SQB, PWB_, bx, bj);
  phase_qkv2<true, 8>(lds, p, sy, WS_X + 2 * XB, WS_SQ + 2 * SQB, 0, WS_O + 3 * XB, POBp, nullptr, bx, bj);
  phase_proj<false, true, 9>(lds, p, sy, WS_O + 3 * XB, WS_X + 2 * XB, 0, 0, nullptr, bx, bj);
  asm volatile(".Lstack_tail:");
  if (blockIdx.x == 0 && threadIdx.x == 0) __hip_atomic_store((GAS u64*)(p.ws + WS_NONCE), sy.nonce + 16, __ATOMIC_RELAXED, __HIP_MEMORY_SCOPE_AGENT);
}

extern "C" void kernel_launch(void* const* d_in, const int* in_sizes, int n_in, void* d_out, int out_size, void* d_ws, size_t ws_size, hipStream_t stream) {
  Params p;
  p.x0 = (const char*)d_in[0]; p.wqA = (const char*)d_in[1]; p.woA = (const char*)d_in[2];
  p.wqB = (const char*)d_in[4]; p.woB = (const char*)d_in[5];
  p.out = (char*)d_out; p.ws = (char*)d_ws;
  k_stack<<<256, 512, 0, stream>>>(p);
}
```
